```python
import torch

def _softmax(x, axis=-1):
    return torch.softmax(x, dim=axis)

def _gelu(x):
    return torch.nn.functional.gelu(x, approximate='tanh')

def _silu(x):
    return torch.nn.functional.silu(x)

def _sigmoid(x):
    return torch.sigmoid(x)

def _layernorm(x, axis=-1, eps=1e-05):
    m = x.mean(dim=axis, keepdim=True)
    v = x.var(dim=axis, keepdim=True, unbiased=False)
    return (x - m) / torch.sqrt(v + eps)

def _scatter_add(tgt, idx, src, axis=0):
    return tgt.clone().index_add_(axis, idx, src)

def _segment_sum(src, seg, n):
    out = torch.zeros((n,) + src.shape[1:], dtype=src.dtype)
    return out.index_add_(0, seg, src)

def _pad(x, pw):
    flat = tuple((v for pair in reversed(pw) for v in pair))
    return torch.nn.functional.pad(x, flat)
kernel_name = 'adaLN_cond__memory_aug_recurrent_mem_tr__dense_activation_silu__prompt'

def setup_inputs():

    _w = lambda a: (lambda t: t.to(torch.bfloat16) if t.is_floating_point() else t)(torch.as_tensor(a))

    def _A_setup():
        B, S, Dm = (1, 2048, 640)
        H, Dh = (32, 128)
        M, seg = (8, 32)
        seg = min(seg, S)
        S = S // seg * seg
        return {'x': _w((lambda *s: torch.randn(*s, dtype=torch.bfloat16) * 0.05)(B, S, Dm)), 'mem0': _w((lambda *s: torch.randn(*s, dtype=torch.bfloat16) * 0.8)(B, M, Dm)), 'Wq': _w((lambda *s: torch.randn(*s, dtype=torch.bfloat16) * 1.6)(Dm, H, Dh) * Dm ** (-0.5)), 'Wk': _w((lambda *s: torch.randn(*s, dtype=torch.bfloat16) * 1.6)(Dm, H, Dh) * Dm ** (-0.5)), 'Wv': _w((lambda *s: torch.randn(*s, dtype=torch.bfloat16) * 0.8)(Dm, H, Dh) * Dm ** (-0.5)), 'Wo': _w((lambda *s: torch.randn(*s, dtype=torch.bfloat16) * 0.8)(H, Dh, Dm) * (H * Dh) ** (-0.5)), 'seg_len': seg}
    _A_in = _A_setup()

    def _B_setup():
        B, S, D, F = (1, 2048, 640, 1728)
        return {'x': _w((lambda *s: torch.randn(*s, dtype=torch.bfloat16) * 0.05)(B, S, D)), 'w1': _w((lambda *s: torch.randn(*s, dtype=torch.bfloat16) * 3.2)(D, F) * D ** (-0.5)), 'w2': _w((lambda *s: torch.randn(*s, dtype=torch.bfloat16) * 0.2)(F, D) * F ** (-0.5))}
    _B_in = _B_setup()
    return {'A_x': _A_in['x'], 'A_mem0': _A_in['mem0'], 'A_Wq': _A_in['Wq'], 'A_Wk': _A_in['Wk'], 'A_Wv': _A_in['Wv'], 'A_Wo': _A_in['Wo'], 'A_seg_len': _A_in['seg_len'], 'B_x': _B_in['x'], 'B_w1': _B_in['w1'], 'B_w2': _B_in['w2'], 'c': _w((lambda *s: torch.randn(*s, dtype=torch.bfloat16) * 0.1)(1, 640) * 8.0), 'w_mod': _w((lambda *s: torch.randn(*s, dtype=torch.bfloat16) * 0.1)(640, 6 * 640) * 640 ** (-0.5) * 8.0)}

def reference(A_x, A_mem0, A_Wq, A_Wk, A_Wv, A_Wo, A_seg_len, B_x, B_w1, B_w2, c, w_mod):

    def _A_ref(x, mem0, Wq, Wk, Wv, Wo, seg_len):
        B, S, Dm = x.shape
        H, Dh = (Wq.shape[1], Wq.shape[2])
        M = mem0.shape[1]
        scale = Dh ** (-0.5)
        n_seg = S // seg_len
        L = M + seg_len
        idx = torch.arange(L)
        mask = (idx[:, None] < M) | (idx[:, None] >= idx[None, :])
        mem = mem0
        outs = []
        for s in range(n_seg):
            xs = x[:, s * seg_len:(s + 1) * seg_len, :].clone()
            h = torch.concatenate([mem, xs], axis=1)
            q = torch.einsum('btd,dhe->bhte', h, Wq)
            k = torch.einsum('btd,dhe->bhte', h, Wk)
            v = torch.einsum('btd,dhe->bhte', h, Wv)
            scores = torch.einsum('bhqe,bhke->bhqk', q, k) * scale
            scores = torch.where(mask[None, None], scores, -1000000000.0)
            p = _softmax(scores, axis=-1)
            o = torch.einsum('bhqk,bhke->bhqe', p, v)
            o = torch.einsum('bhte,hed->btd', o, Wo)
            mem = o[:, :M, :].clone()
            outs.append(o[:, M:, :])
        return torch.concatenate(outs, axis=1)

    def _B_ref(x, w1, w2):
        h = torch.einsum('bsd,df->bsf', x, w1)
        h = _silu(h)
        out = torch.einsum('bsf,fd->bsd', h, w2)
        return out
    x = A_x
    mod = (_silu(c) @ w_mod).reshape(c.shape[0], 6, 1, -1)
    sa, ca, ga, sb, cb, gb = (mod[:, 0].clone(), mod[:, 1].clone(), mod[:, 2].clone(), mod[:, 3].clone(), mod[:, 4].clone(), mod[:, 5].clone())
    h = _layernorm(x, axis=-1) * (1 + ca) + sa
    x = x + ga * _A_ref(h, A_mem0, A_Wq, A_Wk, A_Wv, A_Wo, A_seg_len)
    h = _layernorm(x, axis=-1) * (1 + cb) + sb
    x = x + gb * _B_ref(h, B_w1, B_w2)
    return x
```

```cpp
#include <hip/hip_runtime.h>
#include <stdint.h>

namespace {

typedef unsigned u32;
typedef u32 u32x2 __attribute__((ext_vector_type(2)));
typedef u32 u32x4 __attribute__((ext_vector_type(4)));
typedef u32 u32x8 __attribute__((ext_vector_type(8)));
typedef float f32x2 __attribute__((ext_vector_type(2)));
typedef float f32x4 __attribute__((ext_vector_type(4)));
typedef short s16x4 __attribute__((ext_vector_type(4)));
typedef __bf16 bf16x2 __attribute__((ext_vector_type(2)));
typedef __bf16 bf16x8 __attribute__((ext_vector_type(8)));
typedef __bf16 bf16x16 __attribute__((ext_vector_type(16)));
typedef __amdgpu_buffer_rsrc_t rsrc_t;
#define LAS __attribute__((address_space(3)))
#define DI __device__ __forceinline__

constexpr int DM = 640, NH = 32, DH = 128, NSEG = 64, FF = 1728;
constexpr int HD = NH * DH;
constexpr float SM_C = 0.08838834764831845f * 1.4426950408889634f;
constexpr float LN_EPS = 1e-5f;

constexpr u32 MB = 1u << 20;
constexpr u32 W_STEP = 0;
constexpr u32 W_CTRL = 2 * MB;
constexpr u32 W_FILL = W_CTRL + 65536;
constexpr u32 W_PART = 4 * MB;
constexpr u32 W_ZM = 6 * MB;
constexpr u32 W_H1 = 8 * MB;
constexpr u32 W_X2 = 11 * MB;
constexpr u32 W_KM = 14 * MB;
constexpr u32 W_VM = 18 * MB;
constexpr u32 W_HID = 22 * MB;
constexpr u32 W_KX = 32 * MB;
constexpr u32 W_VX = 48 * MB;
constexpr u32 W_OF = 64 * MB;
constexpr u32 W_DBG = 80 * MB;
constexpr u32 W_END = 84 * MB;
constexpr u32 ST_PM = 16384 + 2048, ST_AL = 16384 + 6144, ST_PROBE = 16384 + 7168;
constexpr u32 C_TICKET = 0, C_TMO = 1024, C_PAR = 1152, C_H1 = 4096, C_K = 12288, C_V = 16384, C_KM = 20480,
              C_VM = 24576, C_O = 28672, C_X2 = 29696, C_HID = 34816, C_MOD = 40960;
constexpr u32 OOB = 0x7FFFFFF0u;
constexpr u32 SENT = 0xFFFFFFFFu;
constexpr u32 SPIN_FAST = 1u << 17, SPIN_SLOW = 1u << 16;
constexpr int LDS_BYTES = 132 * 1024;
constexpr u32 L_MISC = 131 * 1024;

struct Params {
  const void* x; const void* mem0; const void* wq; const void* wk; const void* wv; const void* wo;
  const void* w1; const void* w2; const void* c; const void* wmod;
  void* out; void* ws;
};

DI u32 f2u(float v) { return __float_as_uint(v); }
DI float u2f(u32 v) { return __uint_as_float(v); }
DI u32 cvtpk(float lo, float hi) { f32x2 v = {lo, hi}; bf16x2 b = __builtin_convertvector(v, bf16x2); return __builtin_bit_cast(u32, b); }
DI float bflo(u32 v) { return u2f(v << 16); }
DI float bfhi(u32 v) { return u2f(v & 0xFFFF0000u); }
DI float rbf(float v) { return bflo(cvtpk(v, 0.f)); }
DI u32 guard(u32 v) { return v < 0xFFFFFFFEu ? v : 0xFFFFFFFEu; }
DI u32 rfl(u32 v) { return (u32)__builtin_amdgcn_readfirstlane((int)v); }
DI float ex2(float v) { return __builtin_amdgcn_exp2f(v); }
DI f32x4 mfma32(u32x4 a, u32x4 b, f32x4 c) {
  return __builtin_amdgcn_mfma_f32_16x16x32_bf16(__builtin_bit_cast(bf16x8, a), __builtin_bit_cast(bf16x8, b), c, 0, 0, 0);
}
DI f32x4 mfma16(u32x2 a, u32x2 b, f32x4 c) {
  return __builtin_amdgcn_mfma_f32_16x16x16bf16_1k(__builtin_bit_cast(s16x4, a), __builtin_bit_cast(s16x4, b), c, 0, 0, 0);
}
DI f32x4 smf(u32x4 a, u32x8 b, f32x4 c, u32 idx) {
  return __builtin_amdgcn_smfmac_f32_16x16x64_bf16(__builtin_bit_cast(bf16x8, a), __builtin_bit_cast(bf16x16, b), c, (int)idx, 0, 0);
}
DI u32x2 swap32(u32 a, u32 b) { auto r = __builtin_amdgcn_permlane32_swap(a, b, false, false); return u32x2{r[0], r[1]}; }
DI u32x2 swap16(u32 a, u32 b) { auto r = __builtin_amdgcn_permlane16_swap(a, b, false, false); return u32x2{r[0], r[1]}; }
DI float maxq(float v) {
  u32x2 a = swap16(f2u(v), f2u(v)); v = fmaxf(u2f(a.x), u2f(a.y));
  u32x2 b = swap32(f2u(v), f2u(v)); return fmaxf(u2f(b.x), u2f(b.y));
}
DI float sumq(float v) {
  u32x2 a = swap16(f2u(v), f2u(v)); v = u2f(a.x) + u2f(a.y);
  u32x2 b = swap32(f2u(v), f2u(v)); return u2f(b.x) + u2f(b.y);
}
DI float ror8(float v) { int i = (int)f2u(v); return u2f((u32)__builtin_amdgcn_update_dpp(0, i, 0x128, 0xf, 0xf, true)); }
DI rsrc_t mkrs(const void* p, u32 bytes) { return __builtin_amdgcn_make_buffer_rsrc((void*)p, 0, (int)bytes, 0x00020000); }
DI u32 ld4(rsrc_t r, u32 vo, u32 so) { return __builtin_amdgcn_raw_buffer_load_b32(r, vo, so, 0); }
DI u32x4 ld16(rsrc_t r, u32 vo, u32 so) { return __builtin_amdgcn_raw_buffer_load_b128(r, vo, so, 0); }
DI u32x4 ld16c(rsrc_t r, u32 vo, u32 so) { return __builtin_amdgcn_raw_buffer_load_b128(r, vo, so, 16); }
DI void st4c(rsrc_t r, u32 vo, u32 so, u32 v) { __builtin_amdgcn_raw_buffer_store_b32(v, r, vo, so, 16); }
DI void st8c(rsrc_t r, u32 vo, u32 so, u32x2 v) { __builtin_amdgcn_raw_buffer_store_b64(v, r, vo, so, 16); }
DI void st16c(rsrc_t r, u32 vo, u32 so, u32x4 v) { __builtin_amdgcn_raw_buffer_store_b128(v, r, vo, so, 16); }
DI void bar() { asm volatile("s_waitcnt lgkmcnt(0)\n\ts_barrier" ::: "memory"); }
DI void drain() { asm volatile("s_waitcnt vmcnt(0)" ::: "memory"); }
DI void pin() { __builtin_amdgcn_sched_barrier(0); }
DI void dma(rsrc_t r, u32 vo, u32 so, u32 lds) {
  u32 keep;
  asm volatile("s_nop 4\n\ts_mov_b32 %0, m0\n\ts_mov_b32 m0, %1\n\ts_nop 0\n\tbuffer_load_dwordx4 %2, %3, %4 offen sc1 lds\n\ts_mov_b32 m0, %0"
               : "=&s"(keep) : "s"(lds), "v"(vo), "s"(r), "s"(so) : "memory");
}
DI u32x4 lr16(u32 a) { return *(LAS u32x4*)(uintptr_t)a; }
DI u32x2 lr8(u32 a) { return *(LAS u32x2*)(uintptr_t)a; }
DI u32 lr4(u32 a) { return *(LAS u32*)(uintptr_t)a; }
DI void lw16(u32 a, u32x4 v) { *(LAS u32x4*)(uintptr_t)a = v; }
DI void lw8(u32 a, u32x2 v) { *(LAS u32x2*)(uintptr_t)a = v; }
DI void lw4(u32 a, u32 v) { *(LAS u32*)(uintptr_t)a = v; }
DI u32x2 ltr(u32 a) { s16x4 t = __builtin_amdgcn_ds_read_tr16_b64_v4i16((LAS s16x4*)(uintptr_t)a); return __builtin_bit_cast(u32x2, t); }
DI u32 umax3(u32 a, u32 b, u32 c) { u32 m = a > b ? a : b; return m > c ? m : c; }
DI u32 umax4(u32 m, u32x4 v) { return umax3(umax3(m, v.x, v.y), v.z, v.w); }

struct Ctx {
  rsrc_t ws;
  u32 lane, wave;
  u32 lim;
};
DI void timed_out(Ctx& c) { c.lim = 1; if (c.lane == 0) st4c(c.ws, 0, W_CTRL + C_TMO, 1u); }

DI int wait_ge(Ctx& c, u32 vo, u32 so, int need) {
  int v;
  for (u32 n = 0;; ++n) {
    asm volatile("s_nop 4\n\tbuffer_load_dword %0, %1, %2, %3 offen sc1\n\ts_waitcnt vmcnt(0)" : "=v"(v) : "v"(vo), "s"(c.ws), "s"(so) : "memory");
    if (__builtin_amdgcn_ballot_w64(v < need) == 0) break;
    if (n >= c.lim) { timed_out(c); break; }
    __builtin_amdgcn_s_sleep(2);
  }
  return v;
}

template <int NK> DI void load_dense(rsrc_t w, u32 rowbytes, u32 colbyte0, u32 row0, u32 rowstep, u32 lane, u32x4 (&f)[NK][2], int nvalid) {
  const u32 vo = (8 * (lane >> 4)) * rowbytes + colbyte0 + (lane & 15) * 4;
#pragma unroll
  for (int k = 0; k < NK; ++k) {
    const u32 vk = k < nvalid ? vo : OOB;
#pragma unroll
    for (int i = 0; i < 4; ++i) {
      const u32 r = row0 + k * rowstep + 2 * i;
      const u32 d0 = ld4(w, vk, r * rowbytes), d1 = ld4(w, vk, (r + 1) * rowbytes);
      f[k][0][i] = __builtin_amdgcn_perm(d1, d0, 0x05040100u);
      f[k][1][i] = __builtin_amdgcn_perm(d1, d0, 0x07060302u);
    }
  }
}

DI u32 korder(u32 i, int j) {
  if (j == 0) return i;
  if (j == 1) return i + 4;
  if (j == 2) return 8 + (i & 1);
  if (j < 6) return (i + j - 2) & 3;
  if (j < 9) return 4 + ((i + j - 5) & 3);
  return 8 + ((i + 1) & 1);
}

constexpr u32 LR_MEM = 0, LR_IMQ = 10240, LR_IMK = 12544, LR_KV = 16384;
constexpr u32 IMG_A = 528;
enum { R_X = 0, R_P = 1, R_V = 2 };

#define STAMP(slot) do {} while (0)

template <int ROLE> DI void kv_prefetch(Ctx& c, u32 h, u32 i, int seg, int& kr, int& vr) {
  if (seg >= NSEG - 1) return;
  if (kr < seg) kr = (int)rfl((u32)wait_ge(c, 0, W_CTRL + C_K + h * 128, seg));
  const u32 slot = LR_KV + (u32)(seg % 3) * 16384;
  const u32 src = ((u32)seg * NH + h) * 8192;
  dma(c.ws, c.lane * 16, W_KX + src + i * 2048, slot + i * 2048);
  dma(c.ws, c.lane * 16, W_KX + src + i * 2048 + 1024, slot + i * 2048 + 1024);
  if (ROLE == R_X) {
    if (vr < seg) vr = (int)rfl((u32)wait_ge(c, 0, W_CTRL + C_V + h * 128, seg));
    dma(c.ws, c.lane * 16, W_VX + src + i * 2048, slot + 8192 + i * 2048);
    dma(c.ws, c.lane * 16, W_VX + src + i * 2048 + 1024, slot + 8192 + i * 2048 + 1024);
  }
}

template <int ROLE> DI void rec_role(const Params& p, Ctx& c, const u32 h, const u32 role_id) {
  const u32 lane = c.lane, wave = c.wave, Q = lane >> 4, cc = lane & 15;
  const u32 i = wave & 3;
  const u32 idx = (lane & 8) ? 0xEEEEu : 0x4444u;
  const bool upper = wave >= 4;
  const int nstep = ROLE == R_X ? NSEG - 1 : NSEG;
  const u32 trq = (cc >> 2), trp = cc & 3;
  const u32 tr_lane = 272 * (trq & 1) + 128 * (trp & 1) + 8 * (4 * Q + (trq >> 1));

  if (upper || ROLE == R_P) {
    const void* wp = ROLE == R_V ? p.wv : ((ROLE == R_P && !upper) ? p.wk : p.wq);
    const rsrc_t wr = mkrs(wp, DM * HD * 2);
    u32x8 w[10][2];
    {
      const u32 vo = (8 * (Q & 1) + (Q >> 1)) * (HD * 2) + (h * DH + 32 * i) * 2 + cc * 4;
#pragma unroll
      for (int j = 0; j < 10; ++j) {
        const u32 kap = korder(i, j);
#pragma unroll
        for (int r = 0; r < 8; ++r) {
          const u32 row = 64 * kap + 32 * (r >> 2) + 16 * (r & 1) + 4 * ((r >> 1) & 1);
          const u32 d0 = ld4(wr, vo, row * (HD * 2)), d1 = ld4(wr, vo, (row + 2) * (HD * 2));
          w[j][0][r] = __builtin_amdgcn_perm(d1, d0, 0x05040100u);
          w[j][1][r] = __builtin_amdgcn_perm(d1, d0, 0x07060302u);
        }
      }
    }
    const int pv = wait_ge(c, (lane < 3 ? lane : 0) * 4, W_CTRL + C_PAR, 0);
    const u32 par0 = (u32)(__builtin_amdgcn_readlane(pv, 0) & 1) * 8192;
    const u32 par1 = (u32)(__builtin_amdgcn_readlane(pv, 1) & 1) * 8192;
    const u32 par2 = (u32)(__builtin_amdgcn_readlane(pv, 2) & 1) * 8192;
    const u32 g = 3 * h + i;
    const bool red = ROLE == R_V && i < 3 && g < 80;
    const u32 gmem = ((g >> 3) < 8 ? (g >> 3) * 1024 : 16384 + ((g >> 3) - 8) * 1024) + (g & 7) * 128;
    const u32 pbase = W_PART + g * 16384 + par1, zbase = W_ZM + g * 16384 + par2;
    const u32 vo_pub = (lane & 8) ? OOB : (cc * 16 + (2 * (Q & 1) + (Q >> 1)) * 4);
    const u32x4 sent = {SENT, SENT, SENT, SENT};
    int kr = -1, vr = -1;
    if (ROLE == R_V) {
      if (red) {
#pragma unroll
        for (int k = 0; k < 8; ++k) { st16c(c.ws, lane * 16, pbase + k * 1024, sent); st16c(c.ws, lane * 16, zbase + k * 1024, sent); }
        const rsrc_t mr = mkrs(p.mem0, 8 * DM * 2);
        const u32 vc = 8 * g + 2 * (2 * (Q & 1) + (Q >> 1));
        const u32 col = (vc & ~31u) + 2 * (vc & 15) + ((vc >> 4) & 1);
        const u32 lo = __builtin_amdgcn_raw_buffer_load_b16(mr, ((cc & 7) * DM + col) * 2, 0, 0);
        const u32 hi = __builtin_amdgcn_raw_buffer_load_b16(mr, ((cc & 7) * DM + col + 2) * 2, 0, 0);
        const u32 m0 = guard((lo & 0xFFFFu) | (hi << 16));
        drain();
        st4c(c.ws, vo_pub, W_STEP + par0 + gmem, m0);
      }
    } else if (upper) {
      kv_prefetch<ROLE>(c, h, i, 0, kr, vr);
      kv_prefetch<ROLE>(c, h, i, 1, kr, vr);
      drain();
    }
    bar();
    const u32 img = (ROLE == R_P && !upper) ? LR_IMK : LR_IMQ;
    for (int s = 0; s < nstep; ++s) {
      const u32 sb = W_STEP + (u32)s * 32768 + par0;
      f32x4 a0 = {0.f, 0.f, 0.f, 0.f}, a1 = a0;
      if (upper) {
        u32x4 m0, m1, m2;
        const u32 vo = lane * 16, s0 = sb + i * 1024, s2 = sb + 16384 + (i & 1) * 1024;
        for (u32 n = 0;; ++n) {
          asm volatile("s_nop 4\n\tbuffer_load_dwordx4 %0, %3, %4, %5 offen sc1\n\tbuffer_load_dwordx4 %1, %3, %4, %6 offen sc1\n\t"
                       "buffer_load_dwordx4 %2, %3, %4, %7 offen sc1\n\ts_waitcnt vmcnt(0)"
                       : "=&v"(m0), "=&v"(m1), "=&v"(m2) : "v"(vo), "s"(c.ws), "s"(s0), "s"(s0 + 4096), "s"(s2) : "memory");
          const u32 mx = umax4(umax4(umax4(0u, m0), m1), m2);
          if (__builtin_amdgcn_ballot_w64(mx == SENT) == 0) break;
          if (n >= c.lim) { timed_out(c); break; }
        }
        STAMP(0);
        lw16(LR_MEM + i * 1024 + lane * 16, m0);
        lw16(LR_MEM + (i + 4) * 1024 + lane * 16, m1);
        lw16(LR_MEM + (8 + (i & 1)) * 1024 + lane * 16, m2);
        pin();
        a0 = smf(m0, w[0][0], a0, idx); a1 = smf(m0, w[0][1], a1, idx);
        a0 = smf(m1, w[1][0], a0, idx);
        pin();
        bar();
        u32x4 f[7];
#pragma unroll
        for (int j = 3; j < 10; ++j) f[j - 3] = lr16(LR_MEM + korder(i, j) * 1024 + lane * 16);
        pin();
        a1 = smf(m1, w[1][1], a1, idx);
        a0 = smf(m2, w[2][0], a0, idx); a1 = smf(m2, w[2][1], a1, idx);
#pragma unroll
        for (int j = 3; j < 10; ++j) { a0 = smf(f[j - 3], w[j][0], a0, idx); a1 = smf(f[j - 3], w[j][1], a1, idx); }
      } else {
        bar();
        if (wave == 1 && s > 0 && lane == 0) st4c(c.ws, 0, W_CTRL + C_KM + h * 128, (u32)(s - 1));
#pragma unroll
        for (int j = 0; j < 10; ++j) {
          const u32x4 f = lr16(LR_MEM + korder(i, j) * 1024 + lane * 16);
          a0 = smf(f, w[j][0], a0, idx); a1 = smf(f, w[j][1], a1, idx);
        }
      }
      float sm[4];
#pragma unroll
      for (int r = 0; r < 4; ++r) { const float x0 = a0[r], x1 = a1[r]; const u32x2 t = swap32(f2u(x0), f2u(x1)); sm[r] = u2f(t.x) + u2f(t.y); }
      const u32x2 pk = {cvtpk(sm[0], sm[1]), cvtpk(sm[2], sm[3])};
      lw8(img + i * IMG_A + lane * 8 + (lane >= 32 ? 16 : 0), pk);
      bar();
      STAMP(1);
      if (ROLE == R_V) st8c(c.ws, lane * 8, W_VM + ((u32)s * NH + h) * 2048 + i * 512, pk);
      if (ROLE == R_P && !upper) st8c(c.ws, lane * 8, W_KM + ((u32)s * NH + h) * 2048 + i * 512, pk);

      if (ROLE == R_V) {
        if (red && s + 1 < NSEG) {
          const u32 slot = (u32)(s & 1) * 4096;
          u32x4 z0, z1, z2, z3, q0, q1, q2, q3; u32 al0, al1, al2, al3;
          const u32 vo = lane * 16, va = lane * 4;
          for (u32 n = 0;; ++n) {
            asm volatile("s_nop 4\n\t"
                         "buffer_load_dwordx4 %0, %12, %14, %15 offen sc1\n\tbuffer_load_dwordx4 %1, %12, %14, %15 offen offset:1024 sc1\n\t"
                         "buffer_load_dwordx4 %2, %12, %14, %15 offen offset:2048 sc1\n\tbuffer_load_dwordx4 %3, %12, %14, %15 offen offset:3072 sc1\n\t"
                         "buffer_load_dwordx4 %4, %12, %14, %16 offen sc1\n\tbuffer_load_dwordx4 %5, %12, %14, %16 offen offset:1024 sc1\n\t"
                         "buffer_load_dwordx4 %6, %12, %14, %16 offen offset:2048 sc1\n\tbuffer_load_dwordx4 %7, %12, %14, %16 offen offset:3072 sc1\n\t"
                         "buffer_load_dword %8, %13, %14, %17 offen sc1\n\tbuffer_load_dword %9, %13, %14, %17 offen offset:256 sc1\n\t"
                         "buffer_load_dword %10, %13, %14, %17 offen offset:512 sc1\n\tbuffer_load_dword %11, %13, %14, %17 offen offset:768 sc1\n\t"
                         "s_waitcnt vmcnt(0)"
                         : "=&v"(z0), "=&v"(z1), "=&v"(z2), "=&v"(z3), "=&v"(q0), "=&v"(q1), "=&v"(q2), "=&v"(q3),
                           "=&v"(al0), "=&v"(al1), "=&v"(al2), "=&v"(al3)
                         : "v"(vo), "v"(va), "s"(c.ws), "s"(zbase + slot), "s"(sb + ST_PM), "s"(sb + ST_AL) : "memory");
            u32 mx = umax4(umax4(umax4(umax4(0u, z0), z1), z2), z3);
            mx = umax4(umax4(umax4(umax4(mx, q0), q1), q2), q3);
            mx = umax3(umax3(mx, al0, al1), al2, al3);
            if (__builtin_amdgcn_ballot_w64(mx == SENT) == 0) break;
            if (n >= c.lim) { timed_out(c); break; }
          }
          STAMP(2);
          f32x4 acc = {0.f, 0.f, 0.f, 0.f};
          acc = mfma32(z0, q0, acc); acc = mfma32(z1, q1, acc); acc = mfma32(z2, q2, acc); acc = mfma32(z3, q3, acc);
          const u32 tp = (lane & 7) >> 1;
          u32x4 b0, b1, b2, b3;
#pragma unroll
          for (int r = 0; r < 4; ++r) {
            const bool on = tp == (u32)r;
            b0[r] = on ? al0 : 0u; b1[r] = on ? al1 : 0u; b2[r] = on ? al2 : 0u; b3[r] = on ? al3 : 0u;
          }
          asm volatile("" : "+v"(b0), "+v"(b1), "+v"(b2), "+v"(b3), "+v"(acc));
          for (u32 n = 0;; ++n) {
            asm volatile("s_nop 4\n\t"
                         "buffer_load_dwordx4 %0, %4, %5, %6 offen sc1\n\tbuffer_load_dwordx4 %1, %4, %5, %6 offen offset:1024 sc1\n\t"
                         "buffer_load_dwordx4 %2, %4, %5, %6 offen offset:2048 sc1\n\tbuffer_load_dwordx4 %3, %4, %5, %6 offen offset:3072 sc1\n\t"
                         "s_waitcnt vmcnt(0)"
                         : "=&v"(z0), "=&v"(z1), "=&v"(z2), "=&v"(z3)
                         : "v"(vo), "s"(c.ws), "s"(pbase + slot) : "memory");
            const u32 mx = umax4(umax4(umax4(umax4(0u, z0), z1), z2), z3);
            if (__builtin_amdgcn_ballot_w64(mx == SENT) == 0) break;
            if (n >= c.lim) { timed_out(c); break; }
          }
          STAMP(3);
          acc = mfma32(z0, b0, acc); acc = mfma32(z1, b1, acc); acc = mfma32(z2, b2, acc); acc = mfma32(z3, b3, acc);
          const float c0 = acc[0], c1 = acc[1], c2 = acc[2], c3 = acc[3];
          const u32x2 t0 = swap32(f2u(c0), f2u(c2)), t1 = swap32(f2u(c1), f2u(c3));
          const float r0 = u2f(t0.x) + ror8(u2f(t0.y)), r1 = u2f(t1.x) + ror8(u2f(t1.y));
          st4c(c.ws, vo_pub, sb + 32768 + gmem, guard(cvtpk(r0, r1)));
          STAMP(4);
          pin();
#pragma unroll
          for (int k = 0; k < 4; ++k) { st16c(c.ws, lane * 16, pbase + slot + k * 1024, sent); st16c(c.ws, lane * 16, zbase + slot + k * 1024, sent); }
        }
        bar();
      } else if (upper) {
        kv_prefetch<ROLE>(c, h, i, s + 2, kr, vr);
      } else {
        if (wave == 0 && s + 1 < NSEG) {
          const u32 kv = LR_KV + (u32)(s % 3) * 16384;
          f32x4 S0 = {0.f, 0.f, 0.f, 0.f}, S1 = S0, Sm = S0;
#pragma unroll
          for (int ks = 0; ks < 4; ++ks) {
            const u32x2 qa = ltr(LR_IMQ + ks * IMG_A + tr_lane), qb = ltr(LR_IMQ + ks * IMG_A + tr_lane + 16);
            const u32x2 ka = ltr(LR_IMK + ks * IMG_A + tr_lane), kb = ltr(LR_IMK + ks * IMG_A + tr_lane + 16);
            const u32x4 qf = {qa.x, qa.y, qb.x, qb.y}, kf = {ka.x, ka.y, kb.x, kb.y};
            S0 = mfma32(lr16(kv + ks * 1024 + lane * 16), qf, S0);
            S1 = mfma32(lr16(kv + 4096 + ks * 1024 + lane * 16), qf, S1);
            Sm = mfma32(kf, qf, Sm);
          }
          const float mx = maxq(fmaxf(fmaxf(fmaxf(S0[0], S0[1]), fmaxf(S0[2], S0[3])), fmaxf(fmaxf(S1[0], S1[1]), fmaxf(S1[2], S1[3]))));
          const float mm = maxq(fmaxf(fmaxf(Sm[0], Sm[1]), fmaxf(Sm[2], Sm[3])));
          const float m = fmaxf(mx, mm), mc = m * SM_C;
          float lx = 0.f, em[4];
#pragma unroll
          for (int r = 0; r < 4; ++r) { lx += ex2(fmaf(S0[r], SM_C, -mc)) + ex2(fmaf(S1[r], SM_C, -mc)); em[r] = ex2(fmaf(Sm[r], SM_C, -mc)); }
          if (Q < 2) lx += (em[0] + em[1]) + (em[2] + em[3]);
          const float inv = __builtin_amdgcn_rcpf(sumq(lx));
          const u32x2 pm = {guard(cvtpk(em[0] * inv, em[1] * inv)), guard(cvtpk(em[2] * inv, em[3] * inv))};
          const float al = ex2(fmaf(mx, SM_C, -mc)) * inv;
          const u32 alw = (lane & 1) ? cvtpk(0.f, al) : cvtpk(al, 0.f);
          st8c(c.ws, (Q < 2 && cc < 8) ? cc * 16 + Q * 8 : OOB, sb + ST_PM + h * 128, pm);
          st4c(c.ws, lane < 8 ? lane * 4 : OOB, sb + ST_AL + h * 32, alw);
          STAMP(2);
        }
        drain();
      }
    }
    if (ROLE != R_X) {
      drain();
      bar();
      if (wave == (ROLE == R_P ? 1u : 4u) && lane == 0) st4c(c.ws, 0, W_CTRL + (ROLE == R_P ? C_KM : C_VM) + h * 128, (u32)(NSEG - 1));
    }
  } else {
    const rsrc_t wr = mkrs(p.wo, HD * DM * 2);
    u32x8 w[5][2][2];
    {
      const u32 vo = (h * DH + 8 * (Q >> 1) + 4 * (Q & 1)) * (DM * 2) + (160 * i) * 2 + cc * 4;
#pragma unroll
      for (int pp = 0; pp < 5; ++pp)
#pragma unroll
        for (int kap = 0; kap < 2; ++kap)
#pragma unroll
          for (int r = 0; r < 8; ++r) {
            const u32 row = 64 * kap + 32 * (r & 1) + 16 * (r >> 2) + 2 * ((r >> 1) & 1);
            const u32 d0 = ld4(wr, vo, row * (DM * 2) + pp * 64), d1 = ld4(wr, vo, (row + 1) * (DM * 2) + pp * 64);
            w[pp][0][kap][r] = __builtin_amdgcn_perm(d1, d0, 0x05040100u);
            w[pp][1][kap][r] = __builtin_amdgcn_perm(d1, d0, 0x07060302u);
          }
    }
    const int pv = wait_ge(c, (lane < 3 ? lane : 0) * 4, W_CTRL + C_PAR, 0);
    const u32 parx = (u32)(__builtin_amdgcn_readlane(pv, ROLE == R_X ? 1 : 2) & 1) * 8192;
    const u32 obase = (ROLE == R_X ? W_PART : W_ZM) + parx + (20 * i) * 16384 + h * 128;
    const u32 vo_st = (4 * (Q & 1) + 2 * (Q >> 1) + (cc >> 3)) * 16384 + (cc & 7) * 16;
    const u32 vo_st1 = (Q & 1) ? OOB : vo_st;
    const u32 um = (lane & 8) ? 0xFFFFFFFFu : 0u;
    bar();
    u32x4 kf[2][4];
    if (ROLE == R_X) {
#pragma unroll
      for (int ks = 0; ks < 4; ++ks) { kf[0][ks] = lr16(LR_KV + ks * 1024 + lane * 16); kf[1][ks] = lr16(LR_KV + 4096 + ks * 1024 + lane * 16); }
    }
    for (int s = 0; s < nstep; ++s) {
      bar();
      if (ROLE == R_V && wave == 0 && s > 0 && lane == 0) st4c(c.ws, 0, W_CTRL + C_VM + h * 128, (u32)(s - 1));
      bar();
      if (s + 1 < NSEG) {
        u32x4 A[2];
        if (ROLE == R_X) {
          const u32 kv = LR_KV + (u32)(s % 3) * 16384 + 8192;
          f32x4 S0 = {0.f, 0.f, 0.f, 0.f}, S1 = S0;
          u32x4 qf[4];
#pragma unroll
          for (int ks = 0; ks < 4; ++ks) {
            const u32x2 qa = ltr(LR_IMQ + ks * IMG_A + tr_lane), qb = ltr(LR_IMQ + ks * IMG_A + tr_lane + 16);
            qf[ks] = u32x4{qa.x, qa.y, qb.x, qb.y};
          }
#pragma unroll
          for (int ks = 0; ks < 4; ++ks) { S0 = mfma32(kf[0][ks], qf[ks], S0); S1 = mfma32(kf[1][ks], qf[ks], S1); }
          pin();
          u32x4 vf[4][2];
#pragma unroll
          for (int a = 0; a < 4; ++a)
#pragma unroll
            for (int e = 0; e < 2; ++e) {
              const u32x2 lo = lr8(kv + a * 2048 + lane * 16 + e * 8), hi = lr8(kv + a * 2048 + 1024 + lane * 16 + e * 8);
              vf[a][e] = u32x4{lo.x, lo.y, hi.x, hi.y};
            }
          pin();
          const float mx = maxq(fmaxf(fmaxf(fmaxf(S0[0], S0[1]), fmaxf(S0[2], S0[3])), fmaxf(fmaxf(S1[0], S1[1]), fmaxf(S1[2], S1[3]))));
          const float mc = mx * SM_C;
          float e0[4], e1[4];
#pragma unroll
          for (int r = 0; r < 4; ++r) { e0[r] = ex2(fmaf(S0[r], SM_C, -mc)); e1[r] = ex2(fmaf(S1[r], SM_C, -mc)); }
          const u32x4 P = {cvtpk(e0[0], e0[1]), cvtpk(e0[2], e0[3]), cvtpk(e1[0], e1[1]), cvtpk(e1[2], e1[3])};
          const u32x4 P1 = P & um, P0 = P ^ P1;
#pragma unroll
          for (int kap = 0; kap < 2; ++kap) {
            const f32x4 z = {0.f, 0.f, 0.f, 0.f};
            f32x4 o0 = mfma32(vf[2 * kap][0], P0, z), o1 = mfma32(vf[2 * kap][1], P0, z);
            o0 = mfma32(vf[2 * kap + 1][0], P1, o0); o1 = mfma32(vf[2 * kap + 1][1], P1, o1);
            A[kap] = u32x4{cvtpk(o0[0], o1[0]), cvtpk(o0[1], o1[1]), cvtpk(o0[2], o1[2]), cvtpk(o0[3], o1[3])};
          }
        } else {
#pragma unroll
          for (int kap = 0; kap < 2; ++kap) {
            const u32 ad = LR_IMQ + (2 * kap + (trp >> 1)) * IMG_A + tr_lane;
            const u32x2 va = ltr(ad), vb = ltr(ad + 16);
            A[kap] = u32x4{va.x, va.y, vb.x, vb.y};
          }
        }
        const u32 so = obase + (u32)(s & 1) * 4096;
        u32x2 X[5];
#pragma unroll
        for (int pp = 0; pp < 5; ++pp) {
          const f32x4 z = {0.f, 0.f, 0.f, 0.f};
          f32x4 d0 = smf(A[0], w[pp][0][0], z, idx), d1 = smf(A[0], w[pp][1][0], z, idx);
          d0 = smf(A[1], w[pp][0][1], d0, idx); d1 = smf(A[1], w[pp][1][1], d1, idx);
          float sm[4];
#pragma unroll
          for (int r = 0; r < 4; ++r) { const float x0 = d0[r], x1 = d1[r]; const u32x2 t = swap32(f2u(x0), f2u(x1)); sm[r] = u2f(t.x) + u2f(t.y); }
          X[pp] = u32x2{guard(cvtpk(sm[0], sm[1])), guard(cvtpk(sm[2], sm[3]))};
          if (pp & 1) {
            const u32x2 t0 = swap16(X[pp - 1].x, X[pp].x), t1 = swap16(X[pp - 1].y, X[pp].y);
            st16c(c.ws, vo_st, so + (pp - 1) * 65536, u32x4{t0.x, t1.x, t0.y, t1.y});
          }
        }
        {
          const u32x2 t0 = swap16(X[4].x, X[4].x), t1 = swap16(X[4].y, X[4].y);
          st16c(c.ws, vo_st1, so + 4 * 65536, u32x4{t0.x, t1.x, t0.y, t1.y});
        }
        STAMP(5);
        if (ROLE == R_X) {
          pin();
          const u32 kn = LR_KV + (u32)((s + 1) % 3) * 16384;
#pragma unroll
          for (int ks = 0; ks < 4; ++ks) { kf[0][ks] = lr16(kn + ks * 1024 + lane * 16); kf[1][ks] = lr16(kn + 4096 + ks * 1024 + lane * 16); }
        }
      }
      if (ROLE == R_V) bar();
    }
    if (ROLE == R_V) bar();
  }
}

template <bool ISV> DI void proj_role(const Params& p, Ctx& c, const u32 h) {
  const u32 lane = c.lane, wave = c.wave, a = wave & 3, tt = wave >> 2;
  u32x4 w[20][2];
  load_dense<20>(mkrs(ISV ? p.wv : p.wk, DM * HD * 2), HD * 2, (h * DH + 32 * a) * 2, 0, 32, lane, w, 20);
  wait_ge(c, lane * 128, W_CTRL + C_H1, 0);
#pragma unroll
  for (int b = 0; b < 2; ++b)
#pragma unroll
    for (int k = 0; k < 5; ++k) dma(c.ws, lane * 16, W_H1 + b * 40960 + (wave * 5 + k) * 1024, b * 40960 + (wave * 5 + k) * 1024);
  const u32 flag = W_CTRL + (ISV ? C_V : C_K) + h * 128;
  for (int s = 0; s < NSEG; ++s) {
    if (s <= 2) drain(); else asm volatile("s_waitcnt vmcnt(6)" ::: "memory");
    bar();
    if (wave == 0 && lane == 0 && s > 0) st4c(c.ws, 0, flag, (u32)(s <= 2 ? s - 1 : s - 2));
    {
      const u32 sn = s + 2 < NSEG ? s + 2 : s;
      const u32 slot = (u32)((s + 2) % 3) * 40960;
#pragma unroll
      for (int k = 0; k < 5; ++k) dma(c.ws, lane * 16, W_H1 + sn * 40960 + (wave * 5 + k) * 1024, slot + (wave * 5 + k) * 1024);
    }
    const u32 hb = (u32)(s % 3) * 40960 + tt * 20480 + lane * 16;
    f32x4 d0 = {0.f, 0.f, 0.f, 0.f}, d1 = d0;
#pragma unroll
    for (int ks = 0; ks < 20; ++ks) {
      const u32x4 f = lr16(hb + ks * 1024);
      if (ISV) { d0 = mfma32(f, w[ks][0], d0); d1 = mfma32(f, w[ks][1], d1); }
      else { d0 = mfma32(w[ks][0], f, d0); d1 = mfma32(w[ks][1], f, d1); }
    }
    const u32 dst = ((u32)s * NH + h) * 8192;
    if (ISV) st16c(c.ws, lane * 16, W_VX + dst + a * 2048 + tt * 1024,
                   u32x4{cvtpk(d0[0], d0[1]), cvtpk(d0[2], d0[3]), cvtpk(d1[0], d1[1]), cvtpk(d1[2], d1[3])});
    else st16c(c.ws, lane * 16, W_KX + dst + tt * 4096 + a * 1024,
               u32x4{cvtpk(d0[0], d1[0]), cvtpk(d0[1], d1[1]), cvtpk(d0[2], d1[2]), cvtpk(d0[3], d1[3])});
  }
  drain();
  bar();
  if (wave == 0 && lane == 0) st4c(c.ws, 0, flag, (u32)(NSEG - 1));
}

constexpr u32 LQ_H1 = 0, LQ_KV = 81920, LQ_Q = 114688, LQ_KM = 122880;
DI void q_role(const Params& p, Ctx& c, const u32 h) {
  const u32 lane = c.lane, wave = c.wave, a = wave & 3, tt = wave >> 2, Q = lane >> 4, cc = lane & 15;
  if (h == 0 && wave < 3) {
    const u32 base = wave == 0 ? W_STEP + 63 * 32768 + ST_PROBE : (wave == 1 ? W_PART : W_ZM) + 96 * 16384;
    u32 best0 = 0xFFFFFFFFu, best1 = 0xFFFFFFFFu;
    for (int it = 0; it < 4; ++it) {
      const u32 t0 = (u32)__builtin_amdgcn_s_memtime();
      if (lane == 0) st4c(c.ws, 0, base, 0u);
      drain();
      const u32 t1 = (u32)__builtin_amdgcn_s_memtime();
      if (lane == 0) st4c(c.ws, 0, base + 8192, 0u);
      drain();
      const u32 t2 = (u32)__builtin_amdgcn_s_memtime();
      best0 = min(best0, t1 - t0); best1 = min(best1, t2 - t1);
    }
    if (lane == 0) st4c(c.ws, 0, W_CTRL + C_PAR + wave * 4, best1 < best0 ? 1u : 0u);
  }
  if (h == 0) bar();
  u32x4 w[20][2];
  load_dense<20>(mkrs(p.wq, DM * HD * 2), HD * 2, (h * DH + 32 * a) * 2, 0, 32, lane, w, 20);
  wait_ge(c, lane * 128, W_CTRL + C_H1, 0);
#pragma unroll
  for (int k = 0; k < 5; ++k) dma(c.ws, lane * 16, W_H1 + (wave * 5 + k) * 1024, LQ_H1 + (wave * 5 + k) * 1024);
  const u32 trq = cc >> 2, trp = cc & 3;
  const u32 tr_lane = 256 * (trq & 1) + 128 * (trp & 1) + 8 * (4 * Q + (trq >> 1));
  int kvr = -1;
  for (int s = 0; s < NSEG; ++s) {
    drain();
    bar();
    if (wave == 0 && lane == 0 && s > 0) st4c(c.ws, 0, W_CTRL + C_O + (h >> 2) * 128 + (h & 3) * 4, (u32)(s - 1));
    if (s + 1 < NSEG) {
#pragma unroll
      for (int k = 0; k < 5; ++k)
        dma(c.ws, lane * 16, W_H1 + (u32)(s + 1) * 40960 + (wave * 5 + k) * 1024, LQ_H1 + (u32)((s + 1) & 1) * 40960 + (wave * 5 + k) * 1024);
    }
    {
      if (kvr < s) kvr = (int)rfl((u32)wait_ge(c, 0, W_CTRL + (wave < 4 ? C_K : C_V) + h * 128, s));
      const u32 src = (wave < 4 ? W_KX : W_VX) + ((u32)s * NH + h) * 8192 + a * 2048;
      dma(c.ws, lane * 16, src, LQ_KV + wave * 2048);
      dma(c.ws, lane * 16, src + 1024, LQ_KV + wave * 2048 + 1024);
    }
    const u32 hb = LQ_H1 + (u32)(s & 1) * 40960 + tt * 20480 + lane * 16;
    f32x4 d0 = {0.f, 0.f, 0.f, 0.f}, d1 = d0;
#pragma unroll
    for (int ks = 0; ks < 20; ++ks) {
      const u32x4 f = lr16(hb + ks * 1024);
      d0 = mfma32(w[ks][0], f, d0); d1 = mfma32(w[ks][1], f, d1);
    }
    lw16(LQ_Q + wave * 1024 + lane * 16, u32x4{cvtpk(d0[0], d1[0]), cvtpk(d0[1], d1[1]), cvtpk(d0[2], d1[2]), cvtpk(d0[3], d1[3])});
    if (wave < 4) {
      wait_ge(c, 0, W_CTRL + (wave < 2 ? C_KM : C_VM) + h * 128, s);
      dma(c.ws, lane * 16, (wave < 2 ? W_KM : W_VM) + ((u32)s * NH + h) * 2048 + (wave & 1) * 1024, LQ_KM + wave * 1024);
    }
    drain();
    bar();
    f32x4 S0 = {0.f, 0.f, 0.f, 0.f}, S1 = S0, Sm = S0;
#pragma unroll
    for (int ks = 0; ks < 4; ++ks) {
      const u32x4 qf = lr16(LQ_Q + (tt * 4 + ks) * 1024 + lane * 16);
      const u32x2 ka = ltr(LQ_KM + ks * 512 + tr_lane), kb = ltr(LQ_KM + ks * 512 + tr_lane + 16);
      S0 = mfma32(lr16(LQ_KV + ks * 1024 + lane * 16), qf, S0);
      S1 = mfma32(lr16(LQ_KV + 4096 + ks * 1024 + lane * 16), qf, S1);
      Sm = mfma32(u32x4{ka.x, ka.y, kb.x, kb.y}, qf, Sm);
    }
    const int tok = (int)(16 * tt + cc);
    const float NEG = -3.0e38f;
    float s0[4], s1[4];
#pragma unroll
    for (int r = 0; r < 4; ++r) {
      s0[r] = (int)(4 * Q) + r <= tok ? S0[r] : NEG;
      s1[r] = (int)(16 + 4 * Q) + r <= tok ? S1[r] : NEG;
    }
    float m = fmaxf(fmaxf(fmaxf(s0[0], s0[1]), fmaxf(s0[2], s0[3])), fmaxf(fmaxf(s1[0], s1[1]), fmaxf(s1[2], s1[3])));
    m = maxq(fmaxf(m, fmaxf(fmaxf(Sm[0], Sm[1]), fmaxf(Sm[2], Sm[3]))));
    const float mc = m * SM_C;
    float e0[4], e1[4], em[4], l = 0.f;
#pragma unroll
    for (int r = 0; r < 4; ++r) {
      e0[r] = (int)(4 * Q) + r <= tok ? ex2(fmaf(s0[r], SM_C, -mc)) : 0.f;
      e1[r] = (int)(16 + 4 * Q) + r <= tok ? ex2(fmaf(s1[r], SM_C, -mc)) : 0.f;
      em[r] = Q < 2 ? ex2(fmaf(Sm[r], SM_C, -mc)) : 0.f;
      l += e0[r] + e1[r] + em[r];
    }
    const float inv = __builtin_amdgcn_rcpf(sumq(l));
    const u32x4 P = {cvtpk(e0[0], e0[1]), cvtpk(e0[2], e0[3]), cvtpk(e1[0], e1[1]), cvtpk(e1[2], e1[3])};
    const u32x2 Pm = {cvtpk(em[0], em[1]), cvtpk(em[2], em[3])};
    const u32x4 vlo = lr16(LQ_KV + 8192 + a * 2048 + lane * 16), vhi = lr16(LQ_KV + 8192 + a * 2048 + 1024 + lane * 16);
    const u32x2 vm0 = lr8(LQ_KM + 2048 + a * 512 + (Q & 1) * 128 + cc * 8), vm1 = lr8(LQ_KM + 2048 + a * 512 + 256 + (Q & 1) * 128 + cc * 8);
    const f32x4 z = {0.f, 0.f, 0.f, 0.f};
    f32x4 o0 = mfma32(u32x4{vlo.x, vlo.y, vhi.x, vhi.y}, P, z), o1 = mfma32(u32x4{vlo.z, vlo.w, vhi.z, vhi.w}, P, z);
    o0 = mfma16(vm0, Pm, o0); o1 = mfma16(vm1, Pm, o1);
    st16c(c.ws, lane * 16, W_OF + (((u32)s * 2 + tt) * 128 + h * 4 + a) * 1024,
          u32x4{cvtpk(o0[0] * inv, o1[0] * inv), cvtpk(o0[1] * inv, o1[1] * inv), cvtpk(o0[2] * inv, o1[2] * inv), cvtpk(o0[3] * inv, o1[3] * inv)});
  }
  drain();
  bar();
  if (wave == 0 && lane == 0) st4c(c.ws, 0, W_CTRL + C_O + (h >> 2) * 128 + (h & 3) * 4, (u32)(NSEG - 1));
}

constexpr u32 LP_RED = 0, LP_MOD = 4096;
DI void gather_mod(Ctx& c, u32 first, u32 n4, u32 dst) {
  const u32 t = c.wave * 64 + c.lane;
  if (c.wave * 64 < n4) {
    const u32 tc = t < n4 ? t : n4 - 1;
    u32x4 v;
    for (u32 n = 0;; ++n) {
      asm volatile("s_nop 4\n\tbuffer_load_dwordx4 %0, %1, %2, %3 offen sc1\n\ts_waitcnt vmcnt(0)" : "=v"(v) : "v"(tc * 16), "s"(c.ws), "s"(W_CTRL + C_MOD + first * 4) : "memory");
      if (__builtin_amdgcn_ballot_w64(((v.x | v.y | v.z | v.w) & 0xFFFFu) != 0) == 0) break;
      if (n >= c.lim) { timed_out(c); break; }
      __builtin_amdgcn_s_sleep(1);
    }
    lw16(dst + tc * 16, v);
  }
}

DI void front(const Params& p, Ctx& c, const u32 j) {
  const u32 lane = c.lane, wave = c.wave, Q = lane >> 4, cc = lane & 15, t = wave * 64 + lane;
  const u32 tt = wave >> 2, k0 = (wave & 3) * 5;
  u32x4 wm[10]; u32 cv[10];
  if (j < 60) {
    const rsrc_t wr = mkrs(p.wmod, DM * 3840 * 2), cr = mkrs(p.c, DM * 2);
#pragma unroll
    for (int r = 0; r < 10; ++r) {
      wm[r] = ld16(wr, ((t >> 3) * 10 + r) * 7680 + (t & 7) * 16, j * 128);
      cv[r] = __builtin_amdgcn_raw_buffer_load_b16(cr, ((t >> 3) * 10 + r) * 2, 0, 0);
    }
  }
  u32x4 xf[5];
  {
    const rsrc_t xr = mkrs(p.x, 2048 * DM * 2);
#pragma unroll
    for (int k = 0; k < 5; ++k) xf[k] = ld16(xr, (16 * tt + cc) * (DM * 2) + Q * 16, j * 32 * DM * 2 + (k0 + k) * 64);
  }
  if (j < 60) {
    float acc[8];
#pragma unroll
    for (int e = 0; e < 8; ++e) acc[e] = 0.f;
#pragma unroll
    for (int r = 0; r < 10; ++r) {
      const float cf = bflo(cv[r] & 0xFFFFu);
      const float sc = rbf(cf / (1.f + __expf(-cf)));
#pragma unroll
      for (int e = 0; e < 4; ++e) { acc[2 * e] = fmaf(sc, bflo(wm[r][e]), acc[2 * e]); acc[2 * e + 1] = fmaf(sc, bfhi(wm[r][e]), acc[2 * e + 1]); }
    }
#pragma unroll
    for (int e = 0; e < 8; ++e) { float v = acc[e]; v += ror8(v); acc[e] = sumq(v); }
    if (lane < 8) {
      lw16(LP_RED + wave * 256 + lane * 32, u32x4{f2u(acc[0]), f2u(acc[1]), f2u(acc[2]), f2u(acc[3])});
      lw16(LP_RED + wave * 256 + lane * 32 + 16, u32x4{f2u(acc[4]), f2u(acc[5]), f2u(acc[6]), f2u(acc[7])});
    }
    bar();
    if (wave == 0) {
      float v = 0.f;
#pragma unroll
      for (int k = 0; k < 8; ++k) v += u2f(lr4(LP_RED + k * 256 + lane * 4));
      st4c(c.ws, lane * 4, W_CTRL + C_MOD + j * 256, cvtpk(0.f, v));
    }
  }
  float sum = 0.f;
#pragma unroll
  for (int k = 0; k < 5; ++k)
#pragma unroll
    for (int e = 0; e < 4; ++e) sum += bflo(xf[k][e]) + bfhi(xf[k][e]);
  sum = sumq(sum);
  if (lane < 16) lw4(LP_RED + 2048 + wave * 64 + lane * 4, f2u(sum));
  bar();
  float mean = 0.f;
#pragma unroll
  for (int k = 0; k < 4; ++k) mean += u2f(lr4(LP_RED + 2048 + (tt * 4 + k) * 64 + cc * 4));
  mean *= 1.f / DM;
  float sq = 0.f;
#pragma unroll
  for (int k = 0; k < 5; ++k)
#pragma unroll
    for (int e = 0; e < 4; ++e) { const float u = bflo(xf[k][e]) - mean, v = bfhi(xf[k][e]) - mean; sq = fmaf(u, u, fmaf(v, v, sq)); }
  sq = sumq(sq);
  if (lane < 16) lw4(LP_RED + 2560 + wave * 64 + lane * 4, f2u(sq));
  gather_mod(c, 0, 320, LP_MOD);
  bar();
  float var = 0.f;
#pragma unroll
  for (int k = 0; k < 4; ++k) var += u2f(lr4(LP_RED + 2560 + (tt * 4 + k) * 64 + cc * 4));
  const float rstd = 1.f / sqrtf(var * (1.f / DM) + LN_EPS);
#pragma unroll
  for (int k = 0; k < 5; ++k) {
    const u32 col = (k0 + k) * 32 + Q * 8;
    const u32x4 sa0 = lr16(LP_MOD + col * 4), sa1 = lr16(LP_MOD + col * 4 + 16);
    const u32x4 ca0 = lr16(LP_MOD + 2560 + col * 4), ca1 = lr16(LP_MOD + 2560 + col * 4 + 16);
    u32x4 o;
#pragma unroll
    for (int e = 0; e < 4; ++e) {
      const u32 sl = e < 2 ? sa0[2 * e] : sa1[2 * e - 4], sh = e < 2 ? sa0[2 * e + 1] : sa1[2 * e - 3];
      const u32 cl = e < 2 ? ca0[2 * e] : ca1[2 * e - 4], ch = e < 2 ? ca0[2 * e + 1] : ca1[2 * e - 3];
      o[e] = cvtpk(fmaf((bflo(xf[k][e]) - mean) * rstd, rbf(1.f + u2f(cl)), u2f(sl)),
                   fmaf((bfhi(xf[k][e]) - mean) * rstd, rbf(1.f + u2f(ch)), u2f(sh)));
    }
    st16c(c.ws, lane * 16, W_H1 + j * 40960 + (tt * 20 + k0 + k) * 1024, o);
  }
  drain();
  bar();
  if (wave == 0 && lane == 0) st4c(c.ws, 0, W_CTRL + C_H1 + j * 128, 0u);
}

DI void o_role(const Params& p, Ctx& c, const u32 P, const u32 tt) {
  const u32 lane = c.lane, wave = c.wave, Q = lane >> 4, cc = lane & 15;
  u32x4 w[16][2];
  load_dense<16>(mkrs(p.wo, HD * DM * 2), DM * 2, P * 64, wave * 512, 32, lane, w, 16);
  gather_mod(c, 2 * DM + P * 32, 8, LP_MOD);
  bar();
  const u32x4 ga0 = lr16(LP_MOD + Q * 32), ga1 = lr16(LP_MOD + Q * 32 + 16);
  const rsrc_t xr = mkrs(p.x, 2048 * DM * 2);
  constexpr u32 LO = 16384;
  for (int s = 0; s < NSEG; ++s) {
    wait_ge(c, (lane & 3) * 4, W_CTRL + C_O + wave * 128, s);
    u32x4 f[16];
#pragma unroll
    for (int k = 0; k < 16; ++k) f[k] = ld16c(c.ws, lane * 16, W_OF + (((u32)s * 2 + tt) * 128 + wave * 16 + k) * 1024);
    u32x4 xv;
    if (wave == 0) xv = ld16(xr, (16 * tt + cc) * (DM * 2) + Q * 16, (u32)s * 32 * DM * 2 + P * 64);
    f32x4 d0 = {0.f, 0.f, 0.f, 0.f}, d1 = d0;
#pragma unroll
    for (int k = 0; k < 16; ++k) { d0 = mfma32(w[k][0], f[k], d0); d1 = mfma32(w[k][1], f[k], d1); }
    const u32 lb = LO + (u32)(s & 1) * 16384;
    lw16(lb + wave * 2048 + lane * 16, __builtin_bit_cast(u32x4, d0));
    lw16(lb + wave * 2048 + 1024 + lane * 16, __builtin_bit_cast(u32x4, d1));
    bar();
    if (wave == 0) {
      f32x4 t0 = {0.f, 0.f, 0.f, 0.f}, t1 = t0;
#pragma unroll
      for (int k = 0; k < 8; ++k) {
        t0 += __builtin_bit_cast(f32x4, lr16(lb + k * 2048 + lane * 16));
        t1 += __builtin_bit_cast(f32x4, lr16(lb + k * 2048 + 1024 + lane * 16));
      }
      u32x4 o;
#pragma unroll
      for (int r = 0; r < 4; ++r) {
        const u32 gl = r < 2 ? ga0[2 * r] : ga1[2 * r - 4], gh = r < 2 ? ga0[2 * r + 1] : ga1[2 * r - 3];
        o[r] = cvtpk(fmaf(u2f(gl), t0[r], bflo(xv[r])), fmaf(u2f(gh), t1[r], bfhi(xv[r])));
      }
      st16c(c.ws, lane * 16, W_X2 + (((u32)s * 2 + tt) * 20 + P) * 1024, o);
      drain();
      if (lane == 0) st4c(c.ws, 0, W_CTRL + C_X2 + (tt * 20 + P) * 128, (u32)s);
    }
  }
}

DI void f1_role(const Params& p, Ctx& c, const u32 cg, const u32 tt) {
  const u32 lane = c.lane, wave = c.wave, Q = lane >> 4, cc = lane & 15;
  const u32 pi = cg * 8 + wave;
  u32x4 w[20][2];
  load_dense<20>(mkrs(p.w1, DM * FF * 2), FF * 2, pi * 64, 0, 32, lane, w, pi < 54 ? 20 : 0);
  gather_mod(c, 3 * DM, 320, LP_MOD);
  constexpr u32 LH = 16384;
  const u32 nf = wave < 4 ? 3 : 2;
  for (int s = 0; s < NSEG; ++s) {
    wait_ge(c, (lane < 20 ? lane : 0) * 128, W_CTRL + C_X2 + tt * 20 * 128, s);
    u32x4 xf[3];
    const u32 src = W_X2 + ((u32)s * 2 + tt) * 20480 + wave * 1024;
    xf[0] = ld16c(c.ws, lane * 16, src);
    xf[1] = ld16c(c.ws, lane * 16, src + 8192);
    xf[2] = ld16c(c.ws, wave < 4 ? lane * 16 : OOB, src + 16384);
    float sum = 0.f;
#pragma unroll
    for (int k = 0; k < 3; ++k)
#pragma unroll
      for (int e = 0; e < 4; ++e) sum += bflo(xf[k][e]) + bfhi(xf[k][e]);
    sum = sumq(sum);
    const float cnt = (float)(nf * 32), lm = sum / cnt;
    float sq = 0.f;
#pragma unroll
    for (int k = 0; k < 3; ++k) {
      float q2 = 0.f;
#pragma unroll
      for (int e = 0; e < 4; ++e) { const float u = bflo(xf[k][e]) - lm, v = bfhi(xf[k][e]) - lm; q2 = fmaf(u, u, fmaf(v, v, q2)); }
      if (k < 2 || wave < 4) sq += q2;
    }
    sq = sumq(sq);
    if (lane < 16) lw8(LP_RED + wave * 128 + lane * 8, u32x2{f2u(lm), f2u(sq)});
    bar();
    float lms[8], mean = 0.f, m2 = 0.f;
#pragma unroll
    for (int k = 0; k < 8; ++k) { const u32x2 v = lr8(LP_RED + k * 128 + cc * 8); lms[k] = u2f(v.x); m2 += u2f(v.y); mean += lms[k] * (k < 4 ? 96.f : 64.f); }
    mean *= 1.f / DM;
#pragma unroll
    for (int k = 0; k < 8; ++k) { const float d = lms[k] - mean; m2 = fmaf(d * d, k < 4 ? 96.f : 64.f, m2); }
    const float rstd = 1.f / sqrtf(m2 * (1.f / DM) + LN_EPS);
#pragma unroll
    for (int k = 0; k < 3; ++k) {
      if (k < 2 || wave < 4) {
        const u32 col = (wave + 8 * k) * 32 + Q * 8;
        const u32x4 sa0 = lr16(LP_MOD + col * 4), sa1 = lr16(LP_MOD + col * 4 + 16);
        const u32x4 ca0 = lr16(LP_MOD + 2560 + col * 4), ca1 = lr16(LP_MOD + 2560 + col * 4 + 16);
        u32x4 o;
#pragma unroll
        for (int e = 0; e < 4; ++e) {
          const u32 sl = e < 2 ? sa0[2 * e] : sa1[2 * e - 4], sh = e < 2 ? sa0[2 * e + 1] : sa1[2 * e - 3];
          const u32 cl = e < 2 ? ca0[2 * e] : ca1[2 * e - 4], ch = e < 2 ? ca0[2 * e + 1] : ca1[2 * e - 3];
          o[e] = cvtpk(fmaf((bflo(xf[k][e]) - mean) * rstd, rbf(1.f + u2f(cl)), u2f(sl)),
                       fmaf((bfhi(xf[k][e]) - mean) * rstd, rbf(1.f + u2f(ch)), u2f(sh)));
        }
        lw16(LH + (wave + 8 * k) * 1024 + lane * 16, o);
      }
    }
    bar();
    f32x4 d0 = {0.f, 0.f, 0.f, 0.f}, d1 = d0;
#pragma unroll
    for (int ks = 0; ks < 20; ++ks) {
      const u32x4 f = lr16(LH + ks * 1024 + lane * 16);
      d0 = mfma32(w[ks][0], f, d0); d1 = mfma32(w[ks][1], f, d1);
    }
    u32x4 o;
#pragma unroll
    for (int r = 0; r < 4; ++r) {
      const float u = d0[r], v = d1[r];
      o[r] = cvtpk(u * __builtin_amdgcn_rcpf(1.f + __expf(-u)), v * __builtin_amdgcn_rcpf(1.f + __expf(-v)));
    }
    st16c(c.ws, pi < 54 ? lane * 16 : OOB, W_HID + (((u32)s * 2 + tt) * 54 + pi) * 1024, o);
    drain();
    bar();
    if (wave == 0 && lane == 0) st4c(c.ws, 0, W_CTRL + C_HID + (tt * 7 + cg) * 128, (u32)s);
  }
}

DI void f2_role(const Params& p, Ctx& c, const u32 cg) {
  const u32 lane = c.lane, wave = c.wave, Q = lane >> 4, cc = lane & 15;
  const int nk = wave < 6 ? 7 : 6;
  u32x4 wa[7][2], wb[7][2];
  const rsrc_t wr = mkrs(p.w2, FF * DM * 2);
  load_dense<7>(wr, DM * 2, cg * 128, wave * 32, 256, lane, wa, nk);
  load_dense<7>(wr, DM * 2, cg * 128 + 64, wave * 32, 256, lane, wb, nk);
  gather_mod(c, 5 * DM + cg * 64, 16, LP_MOD);
  bar();
  const u32 rt = (wave >> 1) & 1, rp = wave & 1;
  const u32x4 gb0 = lr16(LP_MOD + rp * 128 + Q * 32), gb1 = lr16(LP_MOD + rp * 128 + Q * 32 + 16);
  const rsrc_t outr = mkrs(p.out, 2048 * DM * 2);
  constexpr u32 LO = 16384;
  for (int s = 0; s < NSEG; ++s) {
    wait_ge(c, (lane < 14 ? lane : 0) * 128, W_CTRL + C_HID, s);
    u32x4 f[2][7];
#pragma unroll
    for (int t = 0; t < 2; ++t)
#pragma unroll
      for (int k = 0; k < 7; ++k)
        f[t][k] = ld16c(c.ws, k < nk ? lane * 16 : OOB, W_HID + (((u32)s * 2 + t) * 54 + wave + 8 * k) * 1024);
    u32x4 xv;
    if (wave < 4) xv = ld16c(c.ws, lane * 16, W_X2 + (((u32)s * 2 + rt) * 20 + cg * 2 + rp) * 1024);
    f32x4 d[2][4];
#pragma unroll
    for (int t = 0; t < 2; ++t) {
#pragma unroll
      for (int n = 0; n < 4; ++n) d[t][n] = f32x4{0.f, 0.f, 0.f, 0.f};
#pragma unroll
      for (int k = 0; k < 7; ++k) {
        d[t][0] = mfma32(wa[k][0], f[t][k], d[t][0]); d[t][1] = mfma32(wa[k][1], f[t][k], d[t][1]);
        d[t][2] = mfma32(wb[k][0], f[t][k], d[t][2]); d[t][3] = mfma32(wb[k][1], f[t][k], d[t][3]);
      }
    }
    bar();
#pragma unroll
    for (int t = 0; t < 2; ++t)
#pragma unroll
      for (int n = 0; n < 4; ++n) lw16(LO + wave * 8192 + (t * 4 + n) * 1024 + lane * 16, __builtin_bit_cast(u32x4, d[t][n]));
    bar();
    if (wave < 4) {
      f32x4 t0 = {0.f, 0.f, 0.f, 0.f}, t1 = t0;
#pragma unroll
      for (int k = 0; k < 8; ++k) {
        t0 += __builtin_bit_cast(f32x4, lr16(LO + k * 8192 + (rt * 4 + rp * 2) * 1024 + lane * 16));
        t1 += __builtin_bit_cast(f32x4, lr16(LO + k * 8192 + (rt * 4 + rp * 2 + 1) * 1024 + lane * 16));
      }
      u32x4 o;
#pragma unroll
      for (int r = 0; r < 4; ++r) {
        const u32 gl = r < 2 ? gb0[2 * r] : gb1[2 * r - 4], gh = r < 2 ? gb0[2 * r + 1] : gb1[2 * r - 3];
        o[r] = cvtpk(fmaf(u2f(gl), t0[r], bflo(xv[r])), fmaf(u2f(gh), t1[r], bfhi(xv[r])));
      }
      __builtin_amdgcn_raw_buffer_store_b128(o, outr, (16 * rt + cc) * (DM * 2) + Q * 16, (u32)s * 32 * DM * 2 + (cg * 2 + rp) * 64, 0);
    }
  }
}

__global__ void __launch_bounds__(512, 2) block_kernel(Params p) {
  extern __shared__ __attribute__((aligned(16))) char lds_raw[];
  Ctx c;
  c.ws = mkrs(p.ws, W_END);
  c.lane = threadIdx.x & 63;
  c.wave = rfl(threadIdx.x >> 6);
  c.lim = SPIN_FAST;
  if (threadIdx.x == 0) {
    u32 x = (u32)__builtin_amdgcn_s_getreg(20 | (3 << 11)) & 7u, role = 0;
    for (int k = 0; k < 8; ++k) {
      const u32 t = __hip_atomic_fetch_add((u32*)((char*)p.ws + W_CTRL + C_TICKET + x * 128), 1u, __ATOMIC_RELAXED, __HIP_MEMORY_SCOPE_AGENT) + 1u;
      if (t < 32u) { role = x * 32 + t; break; }
      x = (x + 1) & 7u;
    }
    *(LAS u32*)(lds_raw + L_MISC) = role;
  }
  __syncthreads();
  const u32 role = rfl(*(LAS u32*)(lds_raw + L_MISC));
  const u32 cls = role >> 5, id = role & 31;
  if (cls == 0) rec_role<R_X>(p, c, id, role);
  else if (cls == 1) rec_role<R_P>(p, c, id, role);
  else if (cls == 2) rec_role<R_V>(p, c, id, role);
  else {
    c.lim = SPIN_SLOW;
    if (cls == 3) q_role(p, c, id);
    else if (cls == 4) proj_role<false>(p, c, id);
    else if (cls == 5) proj_role<true>(p, c, id);
    else {
      const u32 tt = cls - 6;
      front(p, c, id * 2 + tt);
      if (id < 20) o_role(p, c, id, tt);
      else if (id < 27) f1_role(p, c, id - 20, tt);
      else f2_role(p, c, (id - 27) * 2 + tt);
    }
  }
}

}

extern "C" void kernel_launch(void* const* d_in, const int* in_sizes, int n_in, void* d_out, int out_size, void* d_ws,
                              size_t ws_size, hipStream_t stream) {
  (void)in_sizes; (void)n_in; (void)out_size; (void)ws_size;
  char* base = (char*)(((uintptr_t)d_ws + 2 * MB - 1) & ~(uintptr_t)(2 * MB - 1));
  Params p;
  p.x = d_in[0]; p.mem0 = d_in[1]; p.wq = d_in[2]; p.wk = d_in[3]; p.wv = d_in[4]; p.wo = d_in[5];
  p.w1 = d_in[8]; p.w2 = d_in[9]; p.c = d_in[10]; p.wmod = d_in[11];
  p.out = d_out; p.ws = base;
  (void)hipFuncSetAttribute((const void*)block_kernel, hipFuncAttributeMaxDynamicSharedMemorySize, LDS_BYTES);
  (void)hipMemsetAsync(base, 0xFF, W_FILL, stream);
  block_kernel<<<256, 512, LDS_BYTES, stream>>>(p);
}
```

```cpp
#include <hip/hip_runtime.h>
#include <stdint.h>

namespace {

typedef unsigned u32;
typedef unsigned long long u64;
typedef u32 u32x2 __attribute__((ext_vector_type(2)));
typedef u32 u32x4 __attribute__((ext_vector_type(4)));
typedef u32 u32x8 __attribute__((ext_vector_type(8)));
typedef float f32x2 __attribute__((ext_vector_type(2)));
typedef float f32x4 __attribute__((ext_vector_type(4)));
typedef short s16x4 __attribute__((ext_vector_type(4)));
typedef __bf16 bf16x2 __attribute__((ext_vector_type(2)));
typedef __bf16 bf16x8 __attribute__((ext_vector_type(8)));
typedef __bf16 bf16x16 __attribute__((ext_vector_type(16)));
typedef __amdgpu_buffer_rsrc_t rsrc_t;
#define LAS __attribute__((address_space(3)))
#define DI __device__ __forceinline__

constexpr int DM = 640, NH = 32, DH = 128, NSEG = 64, FF = 1728;
constexpr int HD = NH * DH;
constexpr float SM_C = 0.08838834764831845f * 1.4426950408889634f;
constexpr float LN_EPS = 1e-5f;

constexpr u32 MB = 1u << 20;
constexpr u32 W_PART = 0;
constexpr u32 RING_SLOT = 40 * 16384;
constexpr u32 W_MEMA = 120 * 16384;
constexpr u32 W_PA = 123 * 16384;
constexpr u32 W_MEMB = 126 * 16384;
constexpr u32 W_PROBE = 127 * 16384;
constexpr u32 W_CTRL = 2 * MB;
constexpr u32 W_HS = W_CTRL + 65536;
constexpr u32 W_ZM = 4 * MB;
constexpr u32 W_H1 = 8 * MB;
constexpr u32 W_KM = 14 * MB;
constexpr u32 W_VM = 18 * MB;
constexpr u32 W_HID = 22 * MB;
constexpr u32 W_KV = 32 * MB;
constexpr u32 W_OF = 64 * MB;
constexpr u32 W_DBG = 80 * MB;
constexpr u32 W_X2 = 84 * MB;
constexpr u32 X2_HALF = 64 * 40960;
constexpr u32 W_END = 90 * MB;
constexpr u32 C_TMO = 1024, C_O = 2048, C_H1 = 4096, C_KV = 12288, C_HID = 16384,
              C_X2 = 28672, C_MOD = 40960, C_PAR = 56320, C_RI = 20480, C_T = 57344;
constexpr u32 OOB = 0x7FFFFFF0u;
constexpr u32 SENT = 0xFFFFFFFFu;
constexpr u32 SPIN_FAST = 1u << 17, SPIN_SLOW = 1u << 16;
constexpr int LDS_BYTES = 150 * 1024;
constexpr u32 L_STAMP = 146 * 1024;
constexpr u32 L_MISC = 145 * 1024;

struct Params {
  const void* x; const void* mem0; const void* wq; const void* wk; const void* wv; const void* wo;
  const void* w1; const void* w2; const void* c; const void* wmod;
  void* out; void* ws;
};

typedef const __attribute__((address_space(4))) Params* KP;
__device__ __forceinline__ KP fresh(KP k) { asm volatile("" : "+s"(k)); return k; }

DI u32 f2u(float v) { return __float_as_uint(v); }
DI float u2f(u32 v) { return __uint_as_float(v); }
DI u32 cvtpk(float lo, float hi) { f32x2 v = {lo, hi}; bf16x2 b = __builtin_convertvector(v, bf16x2); return __builtin_bit_cast(u32, b); }
DI float bflo(u32 v) { return u2f(v << 16); }
DI float bfhi(u32 v) { return u2f(v & 0xFFFF0000u); }
DI float rbf(float v) { return bflo(cvtpk(v, 0.f)); }
DI u32 guard(u32 v) { return v < 0xFFFFFFFEu ? v : 0xFFFFFFFEu; }
DI u32 hslot(u32 h, int s) { return h * 64 + (((u32)s + 3 * h) & 63); }
DI u32 rfl(u32 v) { return (u32)__builtin_amdgcn_readfirstlane((int)v); }
DI float ex2(float v) { return __builtin_amdgcn_exp2f(v); }
DI f32x4 mfma32(u32x4 a, u32x4 b, f32x4 c) {
  return __builtin_amdgcn_mfma_f32_16x16x32_bf16(__builtin_bit_cast(bf16x8, a), __builtin_bit_cast(bf16x8, b), c, 0, 0, 0);
}
DI f32x4 mfma16(u32x2 a, u32x2 b, f32x4 c) {
  return __builtin_amdgcn_mfma_f32_16x16x16bf16_1k(__builtin_bit_cast(s16x4, a), __builtin_bit_cast(s16x4, b), c, 0, 0, 0);
}
DI f32x4 smf(u32x4 a, u32x8 b, f32x4 c, u32 idx) {
  return __builtin_amdgcn_smfmac_f32_16x16x64_bf16(__builtin_bit_cast(bf16x8, a), __builtin_bit_cast(bf16x16, b), c, (int)idx, 0, 0);
}
DI u32x2 swap32(u32 a, u32 b) { auto r = __builtin_amdgcn_permlane32_swap(a, b, false, false); return u32x2{r[0], r[1]}; }
DI u32x2 swap16(u32 a, u32 b) { auto r = __builtin_amdgcn_permlane16_swap(a, b, false, false); return u32x2{r[0], r[1]}; }
DI float mx2(float a, float b) { return __builtin_elementwise_maximum(a, b); }
DI float mx3(float a, float b, float c) { return mx2(mx2(a, b), c); }
DI float max8(f32x4 a, f32x4 b) { return mx3(mx3(mx3(a[0], a[1], a[2]), a[3], b[0]), mx2(b[1], b[2]), b[3]); }
DI float addf(float a, float b) { float r = a + b; asm("" : "+v"(r)); return r; }
DI float maxq(float v) {
  u32x2 a = swap16(f2u(v), f2u(v)); v = mx2(u2f(a.x), u2f(a.y));
  u32x2 b = swap32(f2u(v), f2u(v)); return mx2(u2f(b.x), u2f(b.y));
}
DI float sumq(float v) {
  u32x2 a = swap16(f2u(v), f2u(v)); v = u2f(a.x) + u2f(a.y);
  u32x2 b = swap32(f2u(v), f2u(v)); return u2f(b.x) + u2f(b.y);
}
DI float ror8(float v) { int i = (int)f2u(v); return u2f((u32)__builtin_amdgcn_update_dpp(0, i, 0x128, 0xf, 0xf, true)); }
DI rsrc_t mkrs(const void* p, u32 bytes) { return __builtin_amdgcn_make_buffer_rsrc((void*)p, 0, (int)bytes, 0x00020000); }
DI u32 ld4(rsrc_t r, u32 vo, u32 so) { return __builtin_amdgcn_raw_buffer_load_b32(r, vo, so, 0); }
DI u32x4 ld16(rsrc_t r, u32 vo, u32 so) { return __builtin_amdgcn_raw_buffer_load_b128(r, vo, so, 0); }
DI u32x4 ld16c(rsrc_t r, u32 vo, u32 so) { return __builtin_amdgcn_raw_buffer_load_b128(r, vo, so, 16); }
DI void st4c(rsrc_t r, u32 vo, u32 so, u32 v) { __builtin_amdgcn_raw_buffer_store_b32(v, r, vo, so, 16); }
DI void st8c(rsrc_t r, u32 vo, u32 so, u32x2 v) { __builtin_amdgcn_raw_buffer_store_b64(v, r, vo, so, 16); }
DI void st16c(rsrc_t r, u32 vo, u32 so, u32x4 v) { __builtin_amdgcn_raw_buffer_store_b128(v, r, vo, so, 16); }
DI void bar() { asm volatile("s_waitcnt lgkmcnt(0)\n\ts_barrier" ::: "memory"); }
DI void drain() { asm volatile("s_waitcnt vmcnt(0)" ::: "memory"); }
DI void pin() { __builtin_amdgcn_sched_barrier(0); }
DI void dma(rsrc_t r, u32 vo, u32 so, u32 lds) {
  u32 keep;
  asm volatile("s_nop 1\n\ts_mov_b32 %0, m0\n\ts_mov_b32 m0, %1\n\ts_nop 0\n\tbuffer_load_dwordx4 %2, %3, %4 offen sc1 lds\n\ts_mov_b32 m0, %0"
               : "=&s"(keep) : "s"(lds), "v"(vo), "s"(r), "s"(so) : "memory");
}
DI void dma4(rsrc_t r, u32 vo, u32 so, u32 lds) {
  u32 keep;
  asm volatile("s_nop 1\n\ts_mov_b32 %0, m0\n\ts_mov_b32 m0, %1\n\ts_nop 0\n\tbuffer_load_dwordx4 %2, %3, %4 offen sc1 lds\n\t"
               "buffer_load_dwordx4 %2, %3, %4 offen offset:1024 sc1 lds\n\tbuffer_load_dwordx4 %2, %3, %4 offen offset:2048 sc1 lds\n\t"
               "buffer_load_dwordx4 %2, %3, %4 offen offset:3072 sc1 lds\n\ts_mov_b32 m0, %0"
               : "=&s"(keep) : "s"(lds), "v"(vo), "s"(r), "s"(so) : "memory");
}
DI void dma2(rsrc_t r, u32 vo, u32 so, u32 lds) {
  u32 keep;
  asm volatile("s_nop 1\n\ts_mov_b32 %0, m0\n\ts_mov_b32 m0, %1\n\ts_nop 0\n\tbuffer_load_dwordx4 %2, %3, %4 offen sc1 lds\n\t"
               "buffer_load_dwordx4 %2, %3, %4 offen offset:1024 sc1 lds\n\ts_mov_b32 m0, %0"
               : "=&s"(keep) : "s"(lds), "v"(vo), "s"(r), "s"(so) : "memory");
}
#define OPP 1
#define W_NT 0
DI void dmaw(rsrc_t r, u32 vo, u32 so, u32 lds) {
  u32 keep;
  if (W_NT) asm volatile("s_nop 1\n\ts_mov_b32 %0, m0\n\ts_mov_b32 m0, %1\n\ts_nop 0\n\tbuffer_load_dwordx4 %2, %3, %4 offen nt lds\n\ts_mov_b32 m0, %0"
               : "=&s"(keep) : "s"(lds), "v"(vo), "s"(r), "s"(so) : "memory");
  else asm volatile("s_nop 1\n\ts_mov_b32 %0, m0\n\ts_mov_b32 m0, %1\n\ts_nop 0\n\tbuffer_load_dwordx4 %2, %3, %4 offen lds\n\ts_mov_b32 m0, %0"
               : "=&s"(keep) : "s"(lds), "v"(vo), "s"(r), "s"(so) : "memory");
}
DI void dma10(rsrc_t r, u32 vo, u32 so, u32 lds) { dma4(r, vo, so, lds); dma4(r, vo, so + 4096, lds + 4096); dma2(r, vo, so + 8192, lds + 8192); }
DI LAS char* lp(u32 a, int o) { return (LAS char*)(uintptr_t)a + o; }
DI u32x4 lr16(u32 a, int o = 0) { return *(LAS u32x4*)lp(a, o); }
DI u32x2 lr8(u32 a, int o = 0) { return *(LAS u32x2*)lp(a, o); }
DI u32 lr4(u32 a, int o = 0) { return *(LAS u32*)lp(a, o); }
DI void lw16(u32 a, u32x4 v, int o = 0) { *(LAS u32x4*)lp(a, o) = v; }
DI void lw8(u32 a, u32x2 v, int o = 0) { *(LAS u32x2*)lp(a, o) = v; }
DI void lw4(u32 a, u32 v, int o = 0) { *(LAS u32*)lp(a, o) = v; }
DI u32x2 ltr(u32 a, int o = 0) { s16x4 t = __builtin_amdgcn_ds_read_tr16_b64_v4i16((LAS s16x4*)lp(a, o)); return __builtin_bit_cast(u32x2, t); }
template <int N> struct ic { static constexpr int value = N; };
template <int N, int I = 0, class F> DI void unroll(F&& f) { if constexpr (I < N) { f(ic<I>{}); unroll<N, I + 1>(f); } }
DI u32x4 empty4() { u32x2 a, b; asm volatile("v_mov_b64 %0, -1\n\tv_mov_b64 %1, -1" : "=v"(a), "=v"(b)); return u32x4{a.x, a.y, b.x, b.y}; }
DI f32x4 zero4() { f32x2 a, b; asm volatile("v_mov_b64 %0, 0\n\tv_mov_b64 %1, 0" : "=v"(a), "=v"(b)); return f32x4{a.x, a.y, b.x, b.y}; }
DI u32 umax3(u32 a, u32 b, u32 c) { u32 m = a > b ? a : b; return m > c ? m : c; }
DI u32 umax4(u32 m, u32x4 v) { return umax3(umax3(m, v.x, v.y), v.z, v.w); }

#define STAMPL(w, s, slot) do {} while (0)
#define STAMPW(w, s, slot) do {} while (0)
#define STAMPE(w, s, slot) STAMPW(w, s, slot)
#define ASTAMP(v)
#define APUT(v, s, slot)
#define STAMP2(w, s, slot) do {} while (0)
#define RSTAMP(x)
#define ROUT

struct Ctx {
  rsrc_t ws;
  void* wsp;
  u32 role;
  u32 lane, wave;
  u32 t0;
  u32 xcc, nonce;
  bool settled;
  u32 lim;
  int left;
};
DI void timed_out(Ctx& c) { c.lim = 1; st4c(c.ws, 0, W_CTRL + C_TMO, 1u); }

template <int SLP = 2> DI int wait_ge(Ctx& c, u32 vo, u32 so, int need) {
  int v;
  so = rfl(so);
  for (u32 n = 0;; ++n) {
    asm volatile("s_nop 4\n\tbuffer_load_dword %0, %1, %2, %3 offen sc1\n\ts_waitcnt vmcnt(0)" : "=v"(v) : "v"(vo), "s"(c.ws), "s"(so) : "memory");
    if (__builtin_amdgcn_ballot_w64(v < need) == 0) break;
    if (n >= c.lim) { timed_out(c); break; }
    __builtin_amdgcn_s_sleep(SLP);
  }
  return v;
}

template <int SLP> DI u32 wait_set(Ctx& c, u32 vo) {
  u32 n = 0;
  for (;; ++n) {
    u32 v;
    asm volatile("buffer_load_dword %0, %1, %2, 0 offen sc1\n\ts_waitcnt vmcnt(0)" : "=v"(v) : "v"(vo), "s"(c.ws) : "memory");
    if (__builtin_amdgcn_ballot_w64(v == SENT) == 0) break;
    if (n >= c.lim) { timed_out(c); break; }
    __builtin_amdgcn_s_sleep(SLP);
  }
  return n;
}

DI void arrive(Ctx& c, u32 n, int r, u32 flag, int value) {
  u32 old = 0;
  if (c.lane == 0) old = __hip_atomic_fetch_add((LAS u32*)lp(L_MISC + (r & 1) * 4, 16), 1u, __ATOMIC_RELAXED, __HIP_MEMORY_SCOPE_WORKGROUP);
  st4c(c.ws, (c.lane == 0 && old == (u32)(r >> 1) * n + n - 1) ? 0 : OOB, flag, (u32)value);
}

[[noreturn]] DI void finish(Ctx& c) {
  __builtin_amdgcn_endpgm();
  __builtin_unreachable();
}

DI void challenge(Ctx& c, u32 v) {
  u32 n = (u32)__builtin_amdgcn_readlane((int)v, 0) + 1;
  if (n == (u32)__builtin_amdgcn_readlane((int)v, 1)) ++n;
  c.nonce = n;
  st4c(c.ws, c.lane == 0 ? 0 : OOB, W_HS + blockIdx.x * 256, n);
  STAMPW(0, 4, 15);
}
DI void hello(Ctx& c) {
  if (c.settled) return;
  u32 v;
  asm volatile("s_nop 4\n\tbuffer_load_dword %0, %1, %2, %3 offen sc1\n\ts_waitcnt vmcnt(0)"
               : "=v"(v) : "v"((c.lane & 1) * 128), "s"(c.ws), "s"(W_HS + blockIdx.x * 256) : "memory");
  challenge(c, v);
}
DI void announce(Ctx& c) {
  st4c(c.ws, c.lane == 0 ? 0 : OOB, W_CTRL + C_T + (blockIdx.x >> 3) * 128 + (blockIdx.x & 7) * 4, c.xcc);
  if (c.lane == 0) __hip_atomic_store((LAS u32*)lp(L_MISC, 0), 1u, __ATOMIC_RELAXED, __HIP_MEMORY_SCOPE_WORKGROUP);
  STAMPW(0, 6, 15);
}
DI void welcome(Ctx& c) {
  if (c.settled) return;
  STAMPW(0, 5, 15);
  for (u32 k = 0;; ++k) {
    u32 v;
    asm volatile("s_nop 4\n\tbuffer_load_dword %0, off, %1, %2 sc1\n\ts_waitcnt vmcnt(0)" : "=v"(v) : "s"(c.ws), "s"(W_HS + blockIdx.x * 256 + 128) : "memory");
    if (rfl(v) == c.nonce) break;
    if (k >= c.lim) { c.lim = 1; break; }
    __builtin_amdgcn_s_sleep(1);
  }
  announce(c);
}
DI void wait_welcome() {
  while (rfl(__hip_atomic_load((LAS u32*)lp(L_MISC, 0), __ATOMIC_RELAXED, __HIP_MEMORY_SCOPE_WORKGROUP)) == 0) __builtin_amdgcn_s_sleep(1);
}
DI bool roles_ok(Ctx& c, u32 other, int& v) {
  const u32 t = C_T + (blockIdx.x >> 3) * 128 + (c.lane & 7) * 4;
  v = wait_ge(c, c.lane < 8 || other == OOB ? t : other, W_CTRL, 0);
  STAMPW(0, 7, 15);
  u32 m = 0;
#pragma unroll
  for (int k = 0; k < 8; ++k) m |= 1u << (__builtin_amdgcn_readlane(v, k) & 7);
  return c.settled || m == 0xFFu;
}
DI u32 gate_word(Ctx& c, bool rows) {
  const u32 t = C_T + (blockIdx.x >> 3) * 128 + (c.lane & 7) * 4;
  return rows ? C_H1 + (c.lane & 3) * 128 : c.lane < 32 ? t : C_PAR + (c.lane - 32) * 4;
}
DI void verdict(Ctx& c, int v) {
  u32 m = 0;
#pragma unroll
  for (int k = 0; k < 8; ++k) m |= 1u << (__builtin_amdgcn_readlane(v, k) & 7);
  const u64 votes = __builtin_amdgcn_ballot_w64((u32)v < f2u(ror8(u2f((u32)v))));
  const u32 r = 8u | (c.settled || m == 0xFFu ? 4u : 0u) | (__builtin_popcount((u32)(votes >> 40) & 0xFFu) > 4 ? 1u : 0u) |
                (__builtin_popcount((u32)(votes >> 56) & 0xFFu) > 4 ? 2u : 0u);
  lw4(L_MISC + (c.settled ? 8 : 4), r);
  STAMPW(0, 7, 15);
}
DI bool rec_gate(Ctx& c, u32& par0, u32& par1) {
  const u32 r = rfl(lr4(L_MISC + (c.settled ? 8 : 4)));
  par0 = (r & 1) * 8192; par1 = (r & 2) * 4096;
  return (r & 4) != 0;
}

#define TOUCH 1
#define T_START_R 0
#define W_AUX 0
#define F1_DELAY 200
#define F1_WAIT 6600
#define G_SLP 2
#define F1_SLP 2
template <int NP, bool REV = false, class A, class S> DI void load_pairs(rsrc_t w, A&& addr, S&& sink) {
  constexpr int CH = 8, DEPTH = 3, NC = (NP + CH - 1) / CH;
  u32 d0[DEPTH + 1][CH], d1[DEPTH + 1][CH];
#pragma unroll
  for (int j = 0; j < NC + DEPTH; ++j) {
    if (j < NC) {
#pragma unroll
      for (int r = 0; r < CH; ++r)
        if (j * CH + r < NP) {
          u32 vo, s0, s1;
          addr((REV ? NC - 1 - j : j) * CH + r, vo, s0, s1);
          d0[j % (DEPTH + 1)][r] = __builtin_amdgcn_raw_buffer_load_b32(w, vo, s0, W_AUX);
          d1[j % (DEPTH + 1)][r] = __builtin_amdgcn_raw_buffer_load_b32(w, vo, s1, W_AUX);
        }
    }
    if (j >= DEPTH) {
      const int k = j - DEPTH;
#pragma unroll
      for (int r = 0; r < CH; ++r)
        if (k * CH + r < NP) {
          u32 lo = __builtin_amdgcn_perm(d1[k % (DEPTH + 1)][r], d0[k % (DEPTH + 1)][r], 0x05040100u);
          u32 hi = __builtin_amdgcn_perm(d1[k % (DEPTH + 1)][r], d0[k % (DEPTH + 1)][r], 0x07060302u);
          asm volatile("" : "+v"(lo), "+v"(hi));
          sink((REV ? NC - 1 - k : k) * CH + r, lo, hi);
        }
    }
    pin();
  }
}

template <int NK, bool REV = false> DI void load_dense(rsrc_t w, u32 rowbytes, u32 colbyte0, u32 row0, u32 rowstep, u32 lane, u32x4 (&f)[NK][2], int nvalid) {
  const u32 vb = (8 * (lane >> 4)) * rowbytes + colbyte0 + (lane & 15) * 4;
  load_pairs<NK * 4, REV>(w,
      [&](int n, u32& vo, u32& s0, u32& s1) {
        const int k = n >> 2, i = n & 3;
        vo = k < nvalid ? vb : OOB;
        s0 = (row0 + k * rowstep + 2 * i) * rowbytes; s1 = s0 + rowbytes;
      },
      [&](int n, u32 lo, u32 hi) { f[n >> 2][0][n & 3] = lo; f[n >> 2][1][n & 3] = hi; });
}

DI void peek(Ctx& c, u32 vo, u32 so, u32 lds) {
  u32 keep;
  asm volatile("s_nop 1\n\ts_mov_b32 %0, m0\n\ts_mov_b32 m0, %1\n\ts_nop 0\n\tbuffer_load_dword %2, %3, %4 offen sc1 lds\n\ts_mov_b32 m0, %0"
               : "=&s"(keep) : "s"(lds), "v"(vo), "s"(c.ws), "s"(so) : "memory");
}
DI u32x2 repack(u32 a, int o0, int o1) {
  const u32 d0 = lr4(a, o0), d1 = lr4(a, o1);
  return u32x2{__builtin_amdgcn_perm(d1, d0, 0x05040100u), __builtin_amdgcn_perm(d1, d0, 0x07060302u)};
}
template <int NREAL, int NI, bool REV, bool ROWS, class I, class T, class L> DI void staged(Ctx& c, I&& issue, T&& take, L&& late) {
  const u32 base = c.wave < 4 ? 65536u : 0u;
  constexpr int SLOT = NI * 4096;
  const u32 box = L_MISC + 256;
  int phase = c.wave != 0 ? 3 : c.settled ? 2 : 1;
  if (c.wave == 0) { lw4(box + c.lane * 4, c.nonce + 1); lw4(box + 256 + c.lane * 4, SENT); }
#pragma unroll
  for (int n = 0; n < 3; ++n) issue(REV ? NREAL - 1 - n : n, base + n * SLOT);
#pragma unroll
  for (int n = 0; n < 10; ++n) {
    const int left = NREAL - 1 - n;
    asm volatile("s_waitcnt vmcnt(%0)" :: "i"((left < 0 ? 0 : left > 2 ? 2 : left) * NI) : "memory");
    bar();
    if (n < 7 && phase == 1) {
      if (rfl(lr4(box)) == c.nonce) { announce(c); phase = 2; }
      else peek(c, 128, W_HS + blockIdx.x * 256, box);
    }
    if (ROWS && n >= 4 && n < 8 && phase == 2) {
      if (__builtin_amdgcn_ballot_w64((int)lr4(box + 256 + c.lane * 4) < 0) == 0) { lw4(L_MISC + (c.settled ? 8 : 4), 1u); phase = 3; }
      else peek(c, gate_word(c, true), W_CTRL, box + 256);
    }
    if (n == 8) late();
    if (n + 3 < NREAL) issue(REV ? NREAL - 4 - n : n + 3, base + ((n + 3) & 3) * SLOT);
    if (n < NREAL) take(REV ? NREAL - 1 - n : n, base + (n & 3) * SLOT);
    pin();
  }
  if (phase == 1) welcome(c);
  if (phase < 3) {
    const int v = wait_ge<1>(c, gate_word(c, ROWS), W_CTRL, 0);
    if (!ROWS) verdict(c, v);
  }
  drain();
  bar();
}
template <class L> DI void stage_dense(Ctx& c, const void* w, u32 h, u32x4 (&f)[20][2], L&& late) {
  const rsrc_t wr = mkrs(w, DM * HD * 2);
  const u32 a = c.wave & 3, Q = c.lane >> 4, cc = c.lane & 15;
  const u32 vo = (16 * a + Q) * (HD * 2) + h * (DH * 2) + cc * 16, ra = 8 * Q * 256 + a * 64 + cc * 4;
  staged<10, 4, false, true>(c,
      [&](int n, u32 slot) {
#pragma unroll
        for (int k = 0; k < 4; ++k) dmaw(wr, vo, (64 * n + 4 * k) * (HD * 2), slot + a * 4096 + k * 1024);
      },
      [&](int n, u32 slot) {
#pragma unroll
        for (int q = 0; q < 8; ++q) {
          u32x2 v = repack(slot + ra, (32 * (q >> 2) + 2 * (q & 3)) * 256, (32 * (q >> 2) + 2 * (q & 3) + 1) * 256);
          asm volatile("" : "+v"(v));
          f[2 * n + (q >> 2)][0][q & 3] = v.x; f[2 * n + (q >> 2)][1][q & 3] = v.y;
        }
      }, late);
}
template <bool REV, bool SPARSE, class P> DI void stage_wo(Ctx& c, const void* w, u32 h, P&& put) {
  const rsrc_t wr = mkrs(w, HD * DM * 2);
  const u32 a = c.wave & 3, Q = c.lane >> 4, cc = c.lane & 15;
  const u32 vo = (h * DH + 8 * a) * (DM * 2) + c.lane * 16, ra = Q * 5120 + a * 320 + cc * 4;
  staged<8, 5, REV, false>(c,
      [&](int n, u32 slot) {
#pragma unroll
        for (int k = 0; k < 5; ++k) dmaw(wr, vo, (32 * (n >> 1) + 4 * (n & 1)) * (DM * 2) + k * 1024, slot + a * 5120 + k * 1024);
      },
      [&](int n, u32 slot) {
#pragma unroll
        for (int q = 0; q < 10; ++q) {
          const int pp = q >> 1, e = q & 1;
          u32x2 v = repack(slot + ra, (SPARSE ? e : 2 * e) * 1280 + pp * 64, (SPARSE ? e + 2 : 2 * e + 1) * 1280 + pp * 64);
          asm volatile("" : "+v"(v));
          put(n, pp, e, v.x, v.y);
        }
      }, [] {});
}

template <int N, int W, class F> DI void lds_stream(u32 base, F&& body) {
  asm volatile("" : "+v"(base));
  u32x4 f[W];
#pragma unroll
  for (int k = 0; k < W; ++k) f[k] = lr16(base, k * 1024);
  pin();
#pragma unroll
  for (int k = 0; k < N; ++k) {
    body(k, f[k % W]);
    if (k + W < N) f[k % W] = lr16(base, (k + W) * 1024);
    pin();
  }
}

DI u32 korder(u32 i, int j) {
  if (j == 0) return i;
  if (j == 1) return i + 4;
  if (j < 5) return (i + j - 1) & 3;
  if (j < 8) return 4 + ((i + j - 4) & 3);
  return (u32)j;
}

constexpr u32 LR_MEM = 0, LR_IMQ = 10240, LR_IMK = 12544, LR_ML = 14848, LR_PEEK = 15360, LR_KV = 16384, LR_WO = 49152;
constexpr u32 IMG_A = 528;
enum { R_X = 0, R_P = 1, R_V = 2 };
#define NO_WORK 0
#define T_Z 1200
#define T_X 1280
#define T_PA 1650
#define T_MEM 2770
#define T_START_Q 20000
#define T_START_POST 28000
#define ADAPT_UP 32
#define ADAPT_DN 1
#define ADAPT_MAX 400
#define T_QWAIT 3000
#define KV_EAGER 8
#define KV_PRIO 1
#define KV_SYNC 10
#define T_START_O 24000
#define T_POFF 0
#define T_PBULK 60
#define NCL 0
#define HK_DELAY 12
#define K_BAR_AT 4

template <int ROLE> DI void kv_prefetch(Ctx& c, u32 h, u32 i, int seg, int& kr) {
  if (seg >= NSEG - 1) return;
  if (kr < seg) kr = (int)rfl((u32)wait_ge<1>(c, 0, W_CTRL + C_KV + h * 128, seg));
  const u32 slot = LR_KV + (u32)(seg & 1) * 16384;
  const u32 src = W_KV + hslot(h, seg) * 16384;
  if (ROLE == R_X) dma4(c.ws, c.lane * 16, src + i * 4096, slot + i * 4096);
  else dma2(c.ws, c.lane * 16, src + i * 2048, slot + i * 2048);
}

DI u32 now() { return (u32)__builtin_amdgcn_s_memtime(); }
DI void pace(u32 t0, u32 d) {
  while ((int)(t0 + d - now()) > 192) __builtin_amdgcn_s_sleep(2);
  while ((int)(t0 + d - now()) > 0) {}
}

DI void poll3(Ctx& c, rsrc_t hs, u32 s0, u32 s1, u32 s2, u32 l0, u32 ra, u32x4& m0, u32x4& m1) {
  u32 t, keep; int n; u32x4 m2;
  asm volatile("s_mov_b32 %[n], %[lim]\n\ts_mov_b32 %[keep], m0\n\tv_mov_b32 %[t], 0\n\ts_nop 1\n\t"
               "1:\n\t"
               "s_mov_b32 m0, %[l0]\n\ts_cmp_ge_u32 %[l0], 0x800\n\t"
               "buffer_load_dwordx4 %[vo], %[rs], %[s0] offen sc1 lds\n\t"
               "s_mov_b32 m0, %[l1]\n\ts_cbranch_scc1 3f\n\t"
               "buffer_load_dwordx4 %[vo], %[rs], %[s1] offen sc1 lds\n\t"
               "s_mov_b32 m0, %[l2]\n\ts_sub_u32 %[n], %[n], 1\n\t"
               "buffer_load_dwordx4 %[vo], %[rs], %[s2] offen sc1 lds\n\t"
               "s_waitcnt vmcnt(2)\n\tds_read_b128 v[244:247], %[ra]\n\t"
               "s_waitcnt vmcnt(1)\n\tds_read_b128 v[248:251], %[ra] offset:4096\n\t"
               "s_waitcnt vmcnt(0)\n\tds_read_b128 v[240:243], %[ra] offset:8192\n\t"
               "s_waitcnt lgkmcnt(1)\n\t"
               "v_max3_u32 %[t], v244, v245, v246\n\tv_max3_u32 %[t], %[t], v247, v248\n\tv_max3_u32 %[t], %[t], v249, v250\n\tv_max_u32 %[t], %[t], v251\n\t"
               "s_waitcnt lgkmcnt(0)\n\t"
               "v_max3_u32 %[t], %[t], v240, v241\n\tv_max3_u32 %[t], %[t], v242, v243\n\t"
               "s_branch 4f\n\t"
               "3:\n\t"
               "buffer_load_dwordx4 %[vo], %[rs], %[s1] offen sc1 lds\n\t"
               "s_sub_u32 %[n], %[n], 1\n\t"
               "s_waitcnt vmcnt(1)\n\tds_read_b128 v[244:247], %[ra]\n\t"
               "s_waitcnt vmcnt(0)\n\tds_read_b128 v[248:251], %[ra] offset:4096\n\t"
               "s_waitcnt lgkmcnt(1)\n\t"
               "v_max3_u32 %[t], v244, v245, v246\n\tv_max_u32 %[t], %[t], v247\n\t"
               "s_waitcnt lgkmcnt(0)\n\t"
               "v_max3_u32 %[t], %[t], v248, v249\n\tv_max3_u32 %[t], %[t], v250, v251\n\t"
               "4:\n\t"
               "v_cmp_eq_u32 vcc, -1, %[t]\n\t"
               "s_cbranch_vccz 2f\n\t"
               "s_cbranch_scc0 1b\n\t"
               "2:\n\ts_barrier\n\ts_mov_b32 m0, %[keep]"
               : "=&{v[244:247]}"(m0), "=&{v[248:251]}"(m1), "=&{v[240:243]}"(m2), [t] "=&v"(t), [n] "=&s"(n), [keep] "=&s"(keep)
               : [vo] "v"(c.lane * 16), [ra] "v"(ra), [rs] "s"(hs), [s0] "s"(s0), [s1] "s"(s1), [s2] "s"(s2),
                 [l0] "s"(l0), [l1] "s"(l0 + 4096), [l2] "s"(l0 + 8192), [lim] "s"(c.lim)
               : "memory", "vcc", "scc");
  c.left = n;
}

#define RED_LDZ "buffer_load_dwordx4 v[188:191], %[vo], %[rs], %[sz] offen sc1\n\tbuffer_load_dwordx4 v[192:195], %[vo], %[rs], %[sz] offen offset:1024 sc1\n\t" \
                "buffer_load_dwordx4 v[196:199], %[vo], %[rs], %[sz] offen offset:2048 sc1\n\tbuffer_load_dwordx4 v[200:203], %[vo], %[rs], %[sz] offen offset:3072 sc1\n\t"
#define RED_LDX "buffer_load_dwordx4 v[204:207], %[vo], %[rs], %[sx] offen sc1\n\tbuffer_load_dwordx4 v[208:211], %[vo], %[rs], %[sx] offen offset:1024 sc1\n\t" \
                "buffer_load_dwordx4 v[212:215], %[vo], %[rs], %[sx] offen offset:2048 sc1\n\tbuffer_load_dwordx4 v[216:219], %[vo], %[rs], %[sx] offen offset:3072 sc1\n\t"
#define RED_LDP "buffer_load_dwordx4 v[220:223], %[vp], %[rs], %[sp] offen sc1\n\tbuffer_load_dwordx4 v[224:227], %[vp], %[rs], %[sp] offen offset:128 sc1\n\t" \
                "buffer_load_dwordx4 v[228:231], %[vp], %[rs], %[sp] offen offset:2048 sc1\n\tbuffer_load_dwordx4 v[232:235], %[vp], %[rs], %[sp] offen offset:2176 sc1\n\t" \
                "buffer_load_dwordx4 v[236:239], %[vp], %[rs], %[sq] offen sc1\n\tbuffer_load_dwordx4 v[240:243], %[vp], %[rs], %[sq] offen offset:128 sc1\n\t" \
                "buffer_load_dwordx4 v[244:247], %[vp], %[rs], %[sq] offen offset:2048 sc1\n\tbuffer_load_dwordx4 v[248:251], %[vp], %[rs], %[sq] offen offset:2176 sc1\n\t"
#define RED_UNTIL(L, T) L ":\n\ts_memtime vcc\n\ts_waitcnt lgkmcnt(0)\n\ts_sub_u32 vcc_lo, vcc_lo, %[" T "]\n\ts_cmp_lt_i32 vcc_lo, 0\n\ts_cbranch_scc1 " L "b\n\t"
#define RED_MMA "s_waitcnt vmcnt(7)\n\tv_mfma_f32_16x16x32_bf16 %[acc], v[188:191], v[220:223], 0\n\t" \
                "s_waitcnt vmcnt(6)\n\tv_mfma_f32_16x16x32_bf16 %[acc], v[204:207], v[224:227], %[acc]\n\t" \
                "s_waitcnt vmcnt(5)\n\tv_mfma_f32_16x16x32_bf16 %[acc], v[192:195], v[228:231], %[acc]\n\t" \
                "s_waitcnt vmcnt(4)\n\tv_mfma_f32_16x16x32_bf16 %[acc], v[208:211], v[232:235], %[acc]\n\t" \
                "s_waitcnt vmcnt(3)\n\tv_mfma_f32_16x16x32_bf16 %[acc], v[196:199], v[236:239], %[acc]\n\t" \
                "s_waitcnt vmcnt(2)\n\tv_mfma_f32_16x16x32_bf16 %[acc], v[212:215], v[240:243], %[acc]\n\t" \
                "s_waitcnt vmcnt(1)\n\tv_mfma_f32_16x16x32_bf16 %[acc], v[200:203], v[244:247], %[acc]\n\t" \
                "s_waitcnt vmcnt(0)\n\tv_mfma_f32_16x16x32_bf16 %[acc], v[216:219], v[248:251], %[acc]\n\t" \
                "s_nop 7"
typedef u32 u32x16 __attribute__((ext_vector_type(16)));
DI u32 umax16(u32 m, u32x16 v) {
#pragma unroll
  for (int k = 0; k < 16; k += 2) m = umax3(m, v[k], v[k + 1]);
  return m;
}
DI int reduce_group(Ctx& c, rsrc_t hs, int s, u32 sz, u32 sx, u32 sp, u32 so_pub, u32 vo_pa, u32 vo_pub, u32 tx_at, u32 tp_at) {
  const u32 lane = c.lane;
  u32x16 Z, X, PA, PB;
  f32x4 acc;
  ASTAMP(tst);
  u32 tq, again;
  asm volatile("s_mov_b32 %[again], 0\n\ts_nop 3\n\t" RED_LDZ RED_UNTIL("5", "tx") RED_LDX RED_UNTIL("6", "tp") RED_LDP
               "s_waitcnt vmcnt(12)\n\tv_max3_u32 %[tq], v188, v192, v196\n\tv_max_u32 %[tq], %[tq], v200\n\tv_cmp_eq_u32 vcc, -1, %[tq]\n\t"
               "s_cbranch_vccz 7f\n\t" RED_LDZ "s_mov_b32 %[again], 1\n\ts_waitcnt vmcnt(12)\n\ts_branch 8f\n\t"
               "7:\n\ts_waitcnt vmcnt(8)\n\t"
               "8:\n\tv_max3_u32 %[tq], v204, v208, v212\n\tv_max_u32 %[tq], %[tq], v216\n\tv_cmp_eq_u32 vcc, -1, %[tq]\n\t"
               "s_cbranch_vccz 9f\n\t" RED_LDX "s_mov_b32 %[again], 1\n\t"
               "9:\n\ts_cmp_eq_u32 %[again], 0\n\ts_cbranch_scc1 1f\n\ts_waitcnt vmcnt(0)\n\t"
               "1:\n\t" RED_MMA
               : "=&{v[188:203]}"(Z), "=&{v[204:219]}"(X), "=&{v[220:235]}"(PA), "=&{v[236:251]}"(PB), [acc] "=&v"(acc), [tq] "=&v"(tq), [again] "=&s"(again)
               : [vo] "v"(lane * 16), [vp] "v"(vo_pa), [rs] "s"(hs), [sz] "s"(sz), [sx] "s"(sx), [sp] "s"(sp), [sq] "s"(sp + 4096),
                 [tx] "s"(tx_at), [tp] "s"(tp_at)
               : "memory", "vcc", "scc");
  u32 res;
  int n = (int)c.lim;
  for (;;) {
    const float c0 = acc[0], c1 = acc[1], c2 = acc[2], c3 = acc[3];
    const u32x2 t0 = swap32(f2u(c0), f2u(c2)), t1 = swap32(f2u(c1), f2u(c3));
    const float r0 = addf(u2f(t0.x), ror8(u2f(t0.y))), r1 = addf(u2f(t1.x), ror8(u2f(t1.y)));
    res = cvtpk(r0, r1);
    if (__builtin_expect(__builtin_amdgcn_ballot_w64(__builtin_isunordered(r0, r1)) == 0, 1)) break;
    u64 nz = __builtin_amdgcn_ballot_w64(umax3(umax3(Z[0], Z[4], Z[8]), Z[12], 0u) == SENT),
        nx = __builtin_amdgcn_ballot_w64(umax3(umax3(X[0], X[4], X[8]), X[12], 0u) == SENT),
        np = __builtin_amdgcn_ballot_w64(umax3(umax3(PA[3], PA[11], PB[3]), PB[11], 0u) == SENT);
    if ((nz | nx | np) == 0) {
      nz = __builtin_amdgcn_ballot_w64(umax16(0u, Z) == SENT); nx = __builtin_amdgcn_ballot_w64(umax16(0u, X) == SENT);
      np = __builtin_amdgcn_ballot_w64(umax16(umax16(0u, PA), PB) == SENT);
    }
    if ((nz | nx | np) == 0 || n < 0) break;
    u32 t0_, t1_, t2_;
    asm volatile("s_nop 4\n\t0:\n\t"
                 "s_cmp_eq_u64 %[nz], 0\n\ts_cbranch_scc1 1f\n\t" RED_LDZ
                 "1:\n\t"
                 "s_cmp_eq_u64 %[nx], 0\n\ts_cbranch_scc1 2f\n\t" RED_LDX
                 "2:\n\t"
                 "s_cmp_eq_u64 %[np], 0\n\ts_cbranch_scc1 3f\n\t" RED_LDP
                 "3:\n\t"
                 "s_sub_u32 %[n], %[n], 1\n\t"
                 "s_waitcnt vmcnt(0)\n\t"
                 "s_cbranch_scc1 4f\n\t"
                 "v_max3_u32 %[t0], v188, v192, v196\n\tv_max3_u32 %[t1], v204, v208, v212\n\tv_max3_u32 %[t2], v223, v227, v231\n\t"
                 "v_max_u32 %[t0], %[t0], v200\n\tv_max_u32 %[t1], %[t1], v216\n\tv_max3_u32 %[t2], %[t2], v235, v239\n\t"
                 "v_cmp_eq_u32 %[nz], -1, %[t0]\n\tv_max3_u32 %[t2], %[t2], v243, v247\n\t"
                 "v_cmp_eq_u32 %[nx], -1, %[t1]\n\tv_max_u32 %[t2], %[t2], v251\n\t"
                 "v_cmp_eq_u32 %[np], -1, %[t2]\n\t"
                 "s_or_b64 vcc, %[nz], %[nx]\n\ts_or_b64 vcc, vcc, %[np]\n\t"
                 "s_cbranch_scc1 0b\n\t"
                 "4:\n\t" RED_MMA
                 : "+{v[188:203]}"(Z), "+{v[204:219]}"(X), "+{v[220:235]}"(PA), "+{v[236:251]}"(PB), [acc] "=&v"(acc),
                   [t0] "=&v"(t0_), [t1] "=&v"(t1_), [t2] "=&v"(t2_), [nz] "+s"(nz), [nx] "+s"(nx), [np] "+s"(np), [n] "+s"(n)
                 : [vo] "v"(lane * 16), [vp] "v"(vo_pa), [rs] "s"(hs), [sz] "s"(sz), [sx] "s"(sx), [sp] "s"(sp), [sq] "s"(sp + 4096)
                 : "memory", "vcc", "scc");
  }
  st4c(hs, vo_pub, so_pub, res);
  STAMPW(4, s, 4);
  const int extra = 2 * ((int)c.lim - n) + (int)again;
  if (n < 0) timed_out(c);
  return extra;
}

DI void clear_slot(rsrc_t hs, u32 so, u32 vo_st, u32 vo_st1) {
  const u32x4 sent = empty4();
  st16c(hs, vo_st, so, sent); st16c(hs, vo_st, so + 4 * 16384, sent); st16c(hs, vo_st1, so + 8 * 16384, sent);
}

template <int ROLE> DI void rings_ready(KP p, Ctx& c, u32 h, u32 par0) {
  const u32 lane = c.lane, Q = lane >> 4, cc = lane & 15;
  const u32 gl = (ROLE == R_V ? 0u : ROLE == R_X ? 8u : 16u) + (h & 7), g = (h >> 3) * 20 + gl;
  if (c.wave == 0 && gl < 20) {
    const u32 gmem = (g < 64 ? W_MEMA + g * 128 : W_MEMB + (g - 64) * 128) + par0, gstep = g < 64 ? 16384 : 2048;
    const u32 vo_pub = (lane & 8) ? OOB : (cc * 16 + (2 * (Q & 1) + (Q >> 1)) * 4);
    const rsrc_t mr = mkrs(fresh(p)->mem0, 8 * DM * 2);
    const u32 vc = 8 * g + 2 * (2 * (Q & 1) + (Q >> 1));
    const u32 col = (vc & ~31u) + 2 * (vc & 15) + ((vc >> 4) & 1);
    const u32 lo = __builtin_amdgcn_raw_buffer_load_b16(mr, ((cc & 7) * DM + col) * 2, 0, 0);
    const u32 hi = __builtin_amdgcn_raw_buffer_load_b16(mr, ((cc & 7) * DM + col + 2) * 2, 0, 0);
    st4c(c.ws, vo_pub, gmem, guard((lo & 0xFFFFu) | (hi << 16)));
    st4c(c.ws, vo_pub, gmem + gstep, SENT);
  }
  if (ROLE != R_X) st16c(c.ws, lane * 16, (ROLE == R_P ? W_KM : W_VM) + hslot(h, c.wave >> 1) * 2048 + (c.wave & 1) * 1024, u32x4{SENT, SENT, SENT, SENT});
  drain();
  u32 old = 0;
  if (lane == 0) old = __hip_atomic_fetch_add((LAS u32*)lp(L_MISC, 12), 1u, __ATOMIC_RELAXED, __HIP_MEMORY_SCOPE_WORKGROUP);
  st4c(c.ws, (lane == 0 && old == 3) ? 0 : OOB, W_CTRL + C_RI + (ROLE * 32 + h) * 64, 0u);
  STAMPW(3, 8, 15);

}

template <int ROLE, bool upper, bool stat> DI void rec_half(KP p, Ctx& c, const u32 h) {
  const u32 lane = c.lane, wave = c.wave, Q = lane >> 4, cc = lane & 15;
  const u32 i = wave & 3;
  const int nstep = ROLE == R_X ? NSEG - 1 : NSEG;
  const u32 trq = (cc >> 2), trp = cc & 3;
  const u32 tr_lane = 272 * (trq & 1) + 128 * (trp & 1) + 8 * (4 * Q + (trq >> 1));

  if constexpr (upper || ROLE == R_P) {
    const u32 idx = (lane & 2) ? 0xEEEEu : 0x4444u;
    const void* wp = ROLE == R_V ? fresh(p)->wv : ((ROLE == R_P && !upper) ? fresh(p)->wk : fresh(p)->wq);
    const rsrc_t wr = mkrs(wp, DM * HD * 2);
    u32x8 w[10][2];
    {
      auto rb = [](int r) { return 32 * (r >> 2) + 16 * (r & 1) + 4 * ((r >> 1) & 1); };
      const u32 vo = Q * (HD * 2) + h * (DH * 2) + cc * 16, col = i * 64 + cc * 4;
      staged<10, 4, OPP && ROLE == R_P && upper, false>(c,
          [&](int n, u32 slot) {
#pragma unroll
            for (int k = 0; k < 4; ++k) {
              const u32 row = n < 8 ? 64 * (4 * (n >> 2) + i) + rb(2 * (n & 3) + (k >> 1)) + 8 * (k & 1) : 64 * n + 16 * i + 4 * k;
              dmaw(wr, vo, row * (HD * 2), slot + i * 4096 + k * 1024);
            }
          },
          [&](int n, u32 slot) {
#pragma unroll
            for (int q = 0; q < 8; ++q) {
              u32x2 v; int j, r;
              if (n < 8) {
                j = (q >> 1) == 0 ? (n >> 2) : 1 + 3 * (n >> 2) + (q >> 1); r = 2 * (n & 3) + (q & 1);
                v = repack(slot + (korder(i, j) & 3) * 4096 + (4 * (Q & 1) + (Q >> 1)) * 256 + col, (q & 1) * 2048, (q & 1) * 2048 + 512);
              } else {
                j = n; r = q;
                v = repack(slot + (8 * (Q & 1) + (Q >> 1)) * 256 + col, rb(q) * 256, rb(q) * 256 + 512);
              }
              asm volatile("" : "+v"(v));
              w[j][0][r] = v.x; w[j][1][r] = v.y;
            }
          }, [] {});
    }
    STAMPW(7, 0, 14);
    u32 par0, par1;
    if (!rec_gate(c, par0, par1)) return;
    const u32 gl = (ROLE == R_V ? 0u : ROLE == R_X ? 8u : 16u) + (h & 7);
    const u32 g = (h >> 3) * 20 + gl;
    const bool red = upper && !stat && i == 0 && gl < 20;
    const u32 gmem = (g < 64 ? W_MEMA + g * 128 : W_MEMB + (g - 64) * 128) + par0, gstep = g < 64 ? 16384 : 2048;
    const u32 goff = (g >> 1) * 16384 + (g & 1) * 4096;
    const u32 vo_pub = (lane & 8) ? OOB : (cc * 16 + (2 * (Q & 1) + (Q >> 1)) * 4);
    int kr = -1;
    if (!upper) {
      if (wave < 2) st4c(c.ws, lane * 4, W_PA + par0 + wave * 16384 + h * 256, SENT);
      rings_ready<ROLE>(p, c, h, par0);
    }
    if (upper) __builtin_amdgcn_s_setprio(2);
    const u32 l16 = lane * 16, rmem = LR_MEM + (2 * Q + ((cc >> 1) & 1)) * 128 + (2 * (cc >> 2) + (cc & 1)) * 16;
    const u32 wimg = ROLE == R_V ? LR_IMQ + i * 512 + cc * 32 + Q * 8
                                 : ((ROLE == R_P && !upper) ? LR_IMK : LR_IMQ) + i * IMG_A + (Q >> 1) * 128 + cc * 8 + (Q & 1) * 4;
    const u32 ra0 = rmem + i * 1024, ra1 = rmem + ((i + 1) & 3) * 1024, ra2 = rmem + ((i + 2) & 3) * 1024, ra3 = rmem + ((i + 3) & 3) * 1024;
    u32 tnext = now();
    if (upper) { lw16(LR_MEM + i * 1024 + l16, u32x4{}); lw16(LR_MEM + i * 1024 + l16, u32x4{}, 4096); lw16(LR_MEM + (i & 1) * 1024 + l16, u32x4{}, 8192); }
    const u32 dcol = (cc & 7) - 4 * (Q & 1);
    const u32 dm0 = dcol == 0 ? 0xFFFFu : dcol == 1 ? 0xFFFF0000u : 0u, dm1 = dcol == 2 ? 0xFFFFu : dcol == 3 ? 0xFFFF0000u : 0u;
    u32 k3 = 2, ring = 2 * RING_SLOT;
    int am = 0, ar = 0, ab = 0;
    for (int s = -1; s < nstep; ++s) {
      const rsrc_t hs = mkrs(c.wsp, s < 0 ? 0u : W_END);
      const u32 kn = k3 == 2 ? 0 : k3 + 1, kp = k3 == 0 ? 2 : k3 - 1;
      f32x4 a0 = zero4(), a1 = zero4();
      u32x4 kx[4];
      u32 sb = W_MEMA + par0 + k3 * 16384 + i * 1024, sb2 = W_MEMB + par0 + k3 * 2048 + (i & 1) * 1024;
      u32 kv = LR_KV + (u32)(s & 1) * 16384 + (i & 1) * 4096 + lane * 16;
      if (stat) asm volatile("" : "+v"(kv));
      u32x4 m0, m1, f[8];
      if (upper) {
        u32 sb1 = sb + 4096;
        asm volatile("" : "+s"(sb), "+s"(sb1), "+s"(sb2));
        pace(tnext, 0);
        ASTAMP(ta);
        poll3(c, hs, sb, sb1, sb2, LR_MEM + i * 1024, ra0, m0, m1);
      } else {
        asm volatile("s_barrier" ::: "memory");
        m0 = lr16(ra0); m1 = lr16(ra0, 4096);
      }
      f[0] = lr16(ra1); f[1] = lr16(ra2); f[2] = lr16(ra3);
      f[3] = lr16(ra1, 4096); f[4] = lr16(ra2, 4096); f[5] = lr16(ra3, 4096);
      f[6] = lr16(rmem, 8192); f[7] = lr16(rmem, 9216);
      pin();
      a0 = smf(m0, w[0][0], a0, idx); a1 = smf(m0, w[0][1], a1, idx);
      a0 = smf(m1, w[1][0], a0, idx); a1 = smf(m1, w[1][1], a1, idx);
      pin();
#pragma unroll
      for (int j = 2; j < 10; ++j) {
        a0 = smf(f[j - 2], w[j][0], a0, idx); a1 = smf(f[j - 2], w[j][1], a1, idx);
        if (stat && j >= 5 && j < 9) kx[j - 5] = lr16(kv, (j - 5) * 1024);
        pin();
        if (!upper && j == K_BAR_AT) { asm volatile("s_barrier" ::: "memory"); pin(); }
      }
      const u32 t2 = upper ? now() - (s < 0 ? 8192 : ROLE == R_P ? T_POFF : 0) : 0;
      pin();
      {
        const u32x2 pk = {cvtpk(addf(a0[0], a0[2]), addf(a0[1], a0[3])), cvtpk(addf(a1[0], a1[2]), addf(a1[1], a1[3]))};
        if (ROLE == R_V) lw8(wimg, pk); else { lw4(wimg, pk.x); lw4(wimg, pk.y, 272); }
      }
      bar();
      pin();
      if (ROLE == R_P && upper) {
        if (stat && s + 1 < NSEG) {
          u32x4 qf[4];
#pragma unroll
          for (int ks = 0; ks < 4; ++ks) {
            const u32x2 qa = ltr(tr_lane, LR_IMQ + ks * IMG_A), qb = ltr(tr_lane, LR_IMQ + ks * IMG_A + 16);
            qf[ks] = u32x4{qa.x, qa.y, qb.x, qb.y};
          }
          pin();
          f32x4 S = {0.f, 0.f, 0.f, 0.f};
#pragma unroll
          for (int ks = 0; ks < 4; ++ks) S = mfma32(kx[ks], qf[ks], S);
          const float m0 = mx2(mx3(S[0], S[1], S[2]), S[3]), m0c = m0 * SM_C;
          float l0 = addf(addf(ex2(fmaf(S[0], SM_C, -m0c)), ex2(fmaf(S[1], SM_C, -m0c))), addf(ex2(fmaf(S[2], SM_C, -m0c)), ex2(fmaf(S[3], SM_C, -m0c))));
          if (i == 3) lw8(LR_ML + lane * 8, u32x2{f2u(m0), f2u(l0)});
          u32 so_pa = W_PA + par0 + k3 * 16384 + h * 256;
          asm volatile("" : "+v"(l0), "+s"(so_pa));
          bar();
          if (i == 2) {
            const u32x2 ml = lr8(LR_ML + lane * 8);
            u32x4 kf[4];
#pragma unroll
            for (int ks = 0; ks < 4; ++ks) {
              const u32x2 ka = ltr(tr_lane, LR_IMK + ks * IMG_A), kb = ltr(tr_lane, LR_IMK + ks * IMG_A + 16);
              kf[ks] = u32x4{ka.x, ka.y, kb.x, kb.y};
            }
            pin();
            const float m1 = u2f(ml.x);
            f32x4 Sm = {0.f, 0.f, 0.f, 0.f};
            u32 ya = f2u(mx2(m0, m1)), yb = ya;
            asm volatile("" : "+v"(ya), "+v"(yb)); pin();
            Sm = mfma32(kf[0], qf[0], Sm); pin();
            { const u32x2 t = swap16(ya, yb); ya = f2u(mx2(u2f(t.x), u2f(t.y))); yb = ya; asm volatile("" : "+v"(ya), "+v"(yb)); } pin();
            Sm = mfma32(kf[1], qf[1], Sm); pin();
            float mx, mxc;
            { const u32x2 t = swap32(ya, yb); mx = mx2(u2f(t.x), u2f(t.y)); mxc = mx * SM_C; asm volatile("" : "+v"(mx), "+v"(mxc)); } pin();
            Sm = mfma32(kf[2], qf[2], Sm); pin();
            float d0 = m0c - mxc, d1 = fmaf(m1, SM_C, -mxc);
            asm volatile("" : "+v"(d0), "+v"(d1)); pin();
            Sm = mfma32(kf[3], qf[3], Sm); pin();
            float lx = fmaf(l0, ex2(d0), u2f(ml.y) * ex2(d1));
            { const u32x2 t = swap32(f2u(lx), f2u(lx)); lx = addf(u2f(t.x), u2f(t.y)); }
            pin();
            float mm = mx2(mx3(Sm[0], Sm[1], Sm[2]), Sm[3]);
            { const u32x2 t = swap16(f2u(mm), f2u(mm)); mm = mx3(u2f(t.x), u2f(t.y), mx); }
            const float mc = mm * SM_C;
            float em[4];
#pragma unroll
            for (int r = 0; r < 4; ++r) em[r] = ex2(fmaf(Sm[r], SM_C, -mc));
            const float ax = ex2(mxc - mc);
            float l = fmaf(lx, ax, addf(addf(em[0], em[1]), addf(em[2], em[3])));
            { const u32x2 t = swap16(f2u(l), f2u(l)); l = addf(u2f(t.x), u2f(t.y)); }
            const float inv = __builtin_amdgcn_rcpf(l);
            const u32 aa = cvtpk(ax * inv, ax * inv);
            const u32 p0 = cvtpk(em[0] * inv, em[1] * inv), p1 = cvtpk(em[2] * inv, em[3] * inv);
            const u32 vo_pa = cc < 8 ? (Q >> 1) * 128 + cc * 16 + (Q & 1) * 8 : OOB;
            st8c(hs, vo_pa, so_pa, u32x2{Q < 2 ? p0 : aa & dm0, Q < 2 ? p1 : aa & dm1});
            STAMPW(6, s, 6);
          }
        } else bar();
      }
      STAMPW(4, s, 1);
      if (upper && c.left < 0) timed_out(c);
      if (upper && ADAPT_UP) am = (int)c.lim - c.left == 2 && s > 1 ? min(am + ADAPT_UP, ADAPT_MAX) : max(am - ADAPT_DN, 0);
      if (ROLE == R_V && i == 3) {
        const u32x4 va = lr16(l16, LR_IMQ), vb = lr16(l16, LR_IMQ + 1024);
        st16c(hs, lane * 16, W_VM + hslot(h, s) * 2048, va);
        st16c(hs, lane * 16 + 1024, W_VM + hslot(h, s) * 2048, vb);
        if (s + 2 < NSEG) {
          const u32x4 sent = empty4();
          st16c(hs, lane * 16, W_VM + hslot(h, s + 2) * 2048, sent);
          st16c(hs, lane * 16 + 1024, W_VM + hslot(h, s + 2) * 2048, sent);
        }
      }
      if (ROLE == R_P && !upper) {
        st8c(hs, lane * 8, W_KM + hslot(h, s) * 2048 + i * 512, lr8(LR_IMK + i * IMG_A + lane * 8 + (lane >= 32 ? 16 : 0)));
        if (wave == 2) st4c(hs, lane * 4, W_PA + par0 + kp * 16384 + h * 256, SENT);
        if (s + 2 < NSEG) st8c(hs, lane * 8, W_KM + hslot(h, s + 2) * 2048 + i * 512, u32x2{SENT, SENT});
        kv_prefetch<ROLE>(c, h, i, s + 1, kr);
        peek(c, 0, W_CTRL + C_KV + h * 128, LR_PEEK);
        if (gl >= 20) {
          __builtin_amdgcn_s_sleep(HK_DELAY);
          const u32x4 sent = empty4();
          const u32 prev = ring == 0 ? 2 * RING_SLOT : ring - RING_SLOT;
#pragma unroll
          for (int k = 0; k < 5 - 2 * NCL; ++k) {
            const u32 gc = (h >> 3) * 20 + 8 * NCL + (gl - 20) * (5 - 2 * NCL) + k, go = (gc >> 1) * 16384 + (gc & 1) * 4096 + prev + i * 1024;
            st16c(hs, l16, W_PART + par0 + go, sent);
            st16c(hs, l16, W_ZM + par1 + go, sent);
          }
        }
        drain();
        kr = max(kr, (int)rfl(lr4(LR_PEEK)));
      }
      if (red && s + 1 < NSEG) {
        st4c(hs, vo_pub, gmem + kp * gstep, SENT);
        pace(t2, T_Z + (ROLE == R_P ? T_PBULK : 0) + ab);
        const int extra = reduce_group(c, hs, s, W_ZM + par1 + goff + ring, W_PART + par0 + goff + ring, W_PA + par0 + k3 * 16384,
                     gmem + kn * gstep, (lane >> 3) * 256 + (lane & 7) * 16, vo_pub, t2 + T_X + (ROLE == R_P ? T_PBULK : 0) + ab, t2 + T_PA + ar);
        if (ADAPT_UP) {
          ar = extra == 2 && s > 0 ? min(ar + ADAPT_UP, ADAPT_MAX) : max(ar - ADAPT_DN, 0);
          ab = extra == 1 && s > 0 ? min(ab + ADAPT_UP / 2, ADAPT_MAX) : max(ab - ADAPT_DN, 0);
        }
      }
      tnext = t2 + T_MEM + am;
      if (upper && s < 0) {
        STAMPW(4, 0, 13);
        wait_ge<3>(c, lane * 64, W_CTRL + C_RI, 0); wait_ge<3>(c, (64 + (lane & 31)) * 64, W_CTRL + C_RI, 0);
        STAMPW(4, 0, 12);
      }
      k3 = kn; ring = kn == 0 ? 0 : ring + RING_SLOT;
    }
  } else if constexpr (ROLE == R_V) {
    const u32 idx = (lane & 2) ? 0xEEEEu : 0x4444u;
    u32x8 w[5][2][2];
    stage_wo<OPP, true>(c, fresh(p)->wo, h, [&](int n, int pp, int e, u32 lo, u32 hi) {
      w[pp][0][n >> 2][2 * (n & 3) + e] = lo; w[pp][1][n >> 2][2 * (n & 3) + e] = hi;
    });
    u32 pary, parx;
    if (!rec_gate(c, pary, parx)) return;
    const u32 obase = W_ZM + parx + (10 * i) * 16384 + h * 128;
    const u32 vo_st = (2 * (Q & 1) + (Q >> 1)) * 16384 + (cc >> 3) * 4096 + (cc & 7) * 16;
    const u32 vo_st1 = (Q & 1) ? OOB : vo_st;
    const u32 ra = LR_IMQ + Q * 256 + cc * 8;
    clear_slot(c.ws, obase, vo_st, vo_st1);
    clear_slot(c.ws, obase + RING_SLOT, vo_st, vo_st1);
    rings_ready<ROLE>(p, c, h, pary);
    u32 slot = 2 * RING_SLOT, prev = RING_SLOT;
    for (int s = -1; s < nstep; ++s) {
      const rsrc_t hs = mkrs(c.wsp, s < 0 ? 0u : W_END);
      f32x4 d[5][2];
#pragma unroll
      for (int pp = 0; pp < 5; ++pp) { d[pp][0] = f32x4{0.f, 0.f, 0.f, 0.f}; d[pp][1] = d[pp][0]; asm volatile("" : "+v"(d[pp][0]), "+v"(d[pp][1])); }
      const u32 so = obase + slot;
      bar();
#pragma unroll
      for (int k = 0; k < NCL; ++k) st16c(hs, vo_st, obase + prev + k * 4 * 16384, empty4());
      __builtin_amdgcn_s_setprio(3);
      bar();
      if (s + 1 < NSEG) {
        const u32x2 va = ltr(ra), vb = ltr(ra, 128), vc = ltr(ra, 1024), vd = ltr(ra, 1152);
        const u32x4 A0 = {va.x, va.y, vb.x, vb.y}, A1 = {vc.x, vc.y, vd.x, vd.y};
        u32 y[5][2]; float ya[5][2], yb[5][2]; u32x2 t0, t1, u0, u1;
        auto S = [&](int pp, int e, int k) { d[pp][e] = smf(k ? A1 : A0, w[pp][e][k], d[pp][e], idx); pin(); };
        auto Aa = [&](int pp, int e) { ya[pp][e] = addf(d[pp][e][0], d[pp][e][2]); pin(); };
        auto Ab = [&](int pp, int e) { yb[pp][e] = addf(d[pp][e][1], d[pp][e][3]); pin(); };
        auto Cv = [&](int pp, int e) { y[pp][e] = cvtpk(ya[pp][e], yb[pp][e]); asm volatile("" : "+v"(y[pp][e])); pin(); };
        pin();
        S(0, 0, 0); S(0, 1, 0); S(0, 0, 1); S(0, 1, 1); S(1, 0, 0); S(1, 1, 0); S(1, 0, 1); S(1, 1, 1);
        S(2, 0, 0); S(2, 1, 0); S(2, 0, 1); Aa(0, 0); Ab(0, 0);
        S(2, 1, 1); Aa(0, 1); Ab(0, 1); Cv(0, 0);
        S(3, 0, 0); Aa(1, 0); Cv(0, 1); Ab(1, 0);
        S(3, 1, 0); Aa(1, 1); Ab(1, 1); Cv(1, 0);
        S(3, 0, 1); Cv(1, 1); t0 = swap32(y[0][0], y[0][1]); pin();
        S(3, 1, 1); Aa(2, 0); t1 = swap32(y[1][0], y[1][1]); pin();
        S(4, 0, 0); Ab(2, 0); u0 = swap16(t0.x, t1.x); pin();
        S(4, 1, 0); u1 = swap16(t0.y, t1.y); pin(); Aa(2, 1);
        st16c(hs, vo_st, so, u32x4{u0.x, u0.y, u1.x, u1.y}); pin();
        S(4, 0, 1); Ab(2, 1);
        S(4, 1, 1); Cv(2, 0); Aa(3, 0); Cv(2, 1); Ab(3, 0); Aa(3, 1); Ab(3, 1); Cv(3, 0); Cv(3, 1);
        t0 = swap32(y[2][0], y[2][1]); pin(); Aa(4, 0); t1 = swap32(y[3][0], y[3][1]); pin(); Ab(4, 0); Aa(4, 1);
        u0 = swap16(t0.x, t1.x); pin(); u1 = swap16(t0.y, t1.y); pin(); Ab(4, 1);
        st16c(hs, vo_st, so + 4 * 16384, u32x4{u0.x, u0.y, u1.x, u1.y}); pin();
        Cv(4, 0); Cv(4, 1);
        t0 = swap32(y[4][0], y[4][1]);
        u0 = swap16(t0.x, t0.x); u1 = swap16(t0.y, t0.y);
        st16c(hs, vo_st1, so + 8 * 16384, u32x4{u0.x, u0.y, u1.x, u1.y});
        __builtin_amdgcn_s_setprio(0);
        STAMPW(0, s, 6);
        prev = slot; slot = slot == 2 * RING_SLOT ? 0 : slot + RING_SLOT;
        drain();
      }
    }

  } else {
    u32x4 w[5][2][2], t[5][2][2];
    {
      stage_wo<false, false>(c, fresh(p)->wo, h, [&](int n, int pp, int e, u32 lo, u32 hi) {
        if (n < 4) { w[pp][0][n >> 1][2 * (n & 1) + e] = lo; w[pp][1][n >> 1][2 * (n & 1) + e] = hi; }
        else { t[pp][0][(n >> 1) - 2][2 * (n & 1) + e] = lo; t[pp][1][(n >> 1) - 2][2 * (n & 1) + e] = hi; }
      });
    }
    u32 parx, pary;
    if (!rec_gate(c, parx, pary)) return;
    const u32 obase = W_PART + parx + (10 * i) * 16384 + h * 128;
    const u32 vo_st = (2 * (Q & 1) + (Q >> 1)) * 16384 + (cc >> 3) * 4096 + (cc & 7) * 16;
    const u32 vo_st1 = (Q & 1) ? OOB : vo_st;
    const u32 um = (lane & 8) ? 0xFFFFFFFFu : 0u;
    clear_slot(c.ws, obase, vo_st, vo_st1);
    clear_slot(c.ws, obase + RING_SLOT, vo_st, vo_st1);
    pin();
#pragma unroll
    for (int k = 0; k < 20; ++k) lw16(LR_WO + i * 20480 + lane * 16, t[k >> 2][(k >> 1) & 1][k & 1], k * 1024);
    rings_ready<ROLE>(p, c, h, parx);
    int kr = -1;
    u32 slot = 2 * RING_SLOT, prev = RING_SLOT;
    for (int s = -1; s < nstep; ++s) {
      const rsrc_t hs = mkrs(c.wsp, s < 0 ? 0u : W_END);
      u32x4 U[5][2], kf[2][4];
      {
        u32 kv = LR_KV + (u32)(s & 1) * 16384 + lane * 16;
        asm volatile("" : "+v"(kv));
        u32x4 vf[2][4];
#pragma unroll
        for (int ks = 0; ks < 4; ++ks) { vf[0][ks] = lr16(kv, 8192 + ks * 1024); vf[1][ks] = lr16(kv, 12288 + ks * 1024); }
        f32x4 e0[2], e1[2];
        auto pack = [&](int t, int half) {
          u32 a = half ? cvtpk(e1[t & 1][0], e1[t & 1][1]) : cvtpk(e0[t & 1][0], e0[t & 1][1]);
          u32 b = half ? cvtpk(e1[t & 1][2], e1[t & 1][3]) : cvtpk(e0[t & 1][2], e0[t & 1][3]);
          asm volatile("" : "+v"(a), "+v"(b));
          U[t >> 1][t & 1][2 * half] = a; U[t >> 1][t & 1][2 * half + 1] = b;
        };
        lds_stream<20, 4>(LR_WO + i * 20480 + lane * 16, [&](int k, u32x4 f) {
          const int t = k >> 1, pp = k >> 2, e = t & 1;
          if ((k & 1) == 0) {
            const f32x4 z = {0.f, 0.f, 0.f, 0.f};
            e0[e] = mfma32(vf[0][0], w[pp][e][0], z); e1[e] = mfma32(vf[1][0], w[pp][e][0], z);
            e0[e] = mfma32(vf[0][1], w[pp][e][1], e0[e]); e1[e] = mfma32(vf[1][1], w[pp][e][1], e1[e]);
            e0[e] = mfma32(vf[0][2], f, e0[e]); e1[e] = mfma32(vf[1][2], f, e1[e]);
          } else {
            e0[e] = mfma32(vf[0][3], f, e0[e]); pin();
            if (t > 0) pack(t - 1, 0);
            pin();
            e1[e] = mfma32(vf[1][3], f, e1[e]); pin();
            if (t > 0) pack(t - 1, 1);
          }
        });
        pack(9, 0); pack(9, 1);
#pragma unroll
        for (int ks = 0; ks < 4; ++ks) { kf[0][ks] = lr16(kv, ks * 1024); kf[1][ks] = lr16(kv, 4096 + ks * 1024); }
      }
      drain();
      if (s >= 0) kr = max(kr, (int)rfl(lr4(LR_PEEK)));
      u32 so0 = obase + slot, so1 = so0 + 2 * 32768, so2 = so0 + 4 * 32768;
      asm volatile("" : "+s"(so0), "+s"(so1), "+s"(so2));
      bar();
      const bool early = kr > s;
#pragma unroll
      for (int k = 0; k < NCL; ++k) st16c(hs, vo_st, obase + prev + k * 4 * 16384, empty4());
      if (early) kv_prefetch<ROLE>(c, h, i, s + 1, kr);
      peek(c, 0, W_CTRL + C_KV + h * 128, LR_PEEK);
      __builtin_amdgcn_s_setprio(3);
      bar();
      f32x4 S0 = {0.f, 0.f, 0.f, 0.f}, S1 = S0;
      u32x4 qf[4];
#pragma unroll
      for (int ks = 0; ks < 4; ++ks) {
        const u32x2 qa = ltr(tr_lane, LR_IMQ + ks * IMG_A), qb = ltr(tr_lane, LR_IMQ + ks * IMG_A + 16);
        qf[ks] = u32x4{qa.x, qa.y, qb.x, qb.y};
      }
      pin();
#pragma unroll
      for (int ks = 0; ks < 4; ++ks) { S0 = mfma32(kf[0][ks], qf[ks], S0); S1 = mfma32(kf[1][ks], qf[ks], S1); }
      pin();
      const float mc = maxq(mx3(mx3(S0[0], S0[1], S0[2]), mx3(S0[3], S1[0], S1[1]), mx2(S1[2], S1[3]))) * SM_C;
      float e0[4], e1[4];
#pragma unroll
      for (int r = 0; r < 4; ++r) { e0[r] = ex2(fmaf(S0[r], SM_C, -mc)); e1[r] = ex2(fmaf(S1[r], SM_C, -mc)); }
      const u32x4 P = {cvtpk(e0[0], e0[1]), cvtpk(e0[2], e0[3]), cvtpk(e1[0], e1[1]), cvtpk(e1[2], e1[3])};
      u32x4 P0 = P & ~um, P1;
      asm volatile("" : "+v"(P0));
      pin();
      f32x4 d[5];
      const f32x4 z = {0.f, 0.f, 0.f, 0.f};
      u32x2 X[5], t0, t1;
      auto Ma = [&](int pp) { d[pp] = mfma32(P0, U[pp][0], z); pin(); };
      auto Mb = [&](int pp) { d[pp] = mfma32(P1, U[pp][1], d[pp]); pin(); };
      auto fin = [&](int pp) { X[pp] = u32x2{cvtpk(d[pp][0], d[pp][1]), cvtpk(d[pp][2], d[pp][3])}; asm volatile("" : "+v"(X[pp])); pin(); };
      Ma(0); P1.x = P.x & um; P1.y = P.y & um; asm volatile("" : "+v"(P1.x), "+v"(P1.y)); pin();
      Ma(1); P1.z = P.z & um; P1.w = P.w & um; asm volatile("" : "+v"(P1.z), "+v"(P1.w)); pin();
      Ma(2); Ma(3); Mb(0); Mb(1); Mb(2); Mb(3); Ma(4); Mb(4);
      fin(0); fin(1); fin(2);
      t0 = swap16(X[0].x, X[1].x); t1 = swap16(X[0].y, X[1].y);
      st16c(hs, vo_st, so0, u32x4{t0.x, t1.x, t0.y, t1.y}); pin();
      fin(3); fin(4);
      t0 = swap16(X[2].x, X[3].x); t1 = swap16(X[2].y, X[3].y);
      st16c(hs, vo_st, so1, u32x4{t0.x, t1.x, t0.y, t1.y}); pin();
      t0 = swap16(X[4].x, X[4].x); t1 = swap16(X[4].y, X[4].y);
      st16c(hs, vo_st1, so2, u32x4{t0.x, t1.x, t0.y, t1.y});
      __builtin_amdgcn_s_setprio(0);
      STAMPW(0, s, 6);
      pin();
      if (early) asm volatile("s_waitcnt vmcnt(4)" ::: "memory");
      else { kv_prefetch<ROLE>(c, h, i, s + 1, kr); drain(); }
      if (lane == 0) __hip_atomic_fetch_add((LAS u32*)lp(L_MISC, 16), 1u, __ATOMIC_RELAXED, __HIP_MEMORY_SCOPE_WORKGROUP);
      while (rfl(__hip_atomic_load((LAS u32*)lp(L_MISC, 16), __ATOMIC_RELAXED, __HIP_MEMORY_SCOPE_WORKGROUP)) < 4u * (u32)(s + 2)) {}
      asm volatile("" ::: "memory");
      prev = slot; slot = slot == 2 * RING_SLOT ? 0 : slot + RING_SLOT;
    }
  }
  finish(c);
}

template <int ROLE> DI void rec_role(KP p, Ctx& c, const u32 h) {
  if (ROLE == R_P && c.wave >= 6) rec_half<ROLE, true, true>(p, c, h);
  else if (c.wave >= 4) rec_half<ROLE, true, false>(p, c, h);
  else rec_half<ROLE, false, false>(p, c, h);
}

constexpr u32 LP_RED = 0, LP_MOD = 4096;
DI void gather_mod(Ctx& c, u32 first, u32 n4, u32 dst) {
  const u32 t = c.wave * 64 + c.lane;
  if (c.wave * 64 < n4) {
    const u32 tc = t < n4 ? t : n4 - 1;
    u32x4 v;
    for (u32 n = 0;; ++n) {
      asm volatile("s_nop 4\n\tbuffer_load_dwordx4 %0, %1, %2, %3 offen sc1\n\ts_waitcnt vmcnt(0)" : "=v"(v) : "v"(tc * 16), "s"(c.ws), "s"(W_CTRL + C_MOD + first * 4) : "memory");
      if (__builtin_amdgcn_ballot_w64(((v.x | v.y | v.z | v.w) & 0xFFFFu) != 0) == 0) break;
      if (n >= c.lim) { timed_out(c); break; }
      __builtin_amdgcn_s_sleep(1);
    }
    lw16(dst + tc * 16, v);
  }
}

DI void mod_slab(KP p, Ctx& c, const u32 slab, bool first) {
  const u32 lane = c.lane, wave = c.wave, t = wave * 64 + lane;
  u32x4 wm[10]; u32 cv[10];
  const rsrc_t wr = mkrs(fresh(p)->wmod, DM * 3840 * 2), cr = mkrs(fresh(p)->c, DM * 2);
#pragma unroll
  for (int r = 0; r < 10; ++r) {
    wm[r] = ld16(wr, ((t >> 3) + 64 * r) * 7680 + (t & 7) * 16, slab * 128);
    cv[r] = __builtin_amdgcn_raw_buffer_load_b16(cr, ((t >> 3) + 64 * r) * 2, 0, 0);
  }
  float acc[8];
#pragma unroll
  for (int e = 0; e < 8; ++e) acc[e] = 0.f;
#pragma unroll
  for (int r = 0; r < 10; ++r) {
    const float cf = bflo(cv[r] & 0xFFFFu);
    const float sc = rbf(cf * __builtin_amdgcn_rcpf(1.f + __expf(-cf)));
#pragma unroll
    for (int e = 0; e < 4; ++e) { acc[2 * e] = fmaf(sc, bflo(wm[r][e]), acc[2 * e]); acc[2 * e + 1] = fmaf(sc, bfhi(wm[r][e]), acc[2 * e + 1]); }
  }
#pragma unroll
  for (int e = 0; e < 8; ++e) { float v = acc[e]; v += ror8(v); acc[e] = sumq(v); }
  if (lane < 8) {
    lw16(LP_RED + wave * 256 + lane * 32, u32x4{f2u(acc[0]), f2u(acc[1]), f2u(acc[2]), f2u(acc[3])});
    lw16(LP_RED + wave * 256 + lane * 32 + 16, u32x4{f2u(acc[4]), f2u(acc[5]), f2u(acc[6]), f2u(acc[7])});
  }
  bar();
  if (wave == 0) {
    float v = 0.f;
#pragma unroll
    for (int k = 0; k < 8; ++k) v += u2f(lr4(LP_RED + k * 256 + lane * 4));
    v = rbf(v);
    const u32 part = slab / 10;
    if (part == 1 || part == 4) v = rbf(1.f + v);
    if (first) welcome(c);
    st4c(c.ws, lane * 4, W_CTRL + C_MOD + slab * 256, f2u(v));
  }
  bar();
}

template <class S> DI void ln_segment(Ctx& c, u32x4 (&xf)[5], u32 red, u32 mod, S&& sink) {
  const u32 lane = c.lane, wave = c.wave, Q = lane >> 4, cc = lane & 15;
  const u32 tt = wave >> 2, k0 = (wave & 3) * 5;
  float sum = 0.f;
#pragma unroll
  for (int k = 0; k < 5; ++k)
#pragma unroll
    for (int e = 0; e < 4; ++e) sum += bflo(xf[k][e]) + bfhi(xf[k][e]);
  sum = sumq(sum);
  if (lane < 16) lw4(red + wave * 64 + lane * 4, f2u(sum));
  bar();
  float mean = 0.f;
#pragma unroll
  for (int k = 0; k < 4; ++k) mean += u2f(lr4(red + (tt * 4 + k) * 64 + cc * 4));
  mean *= 1.f / DM;
  asm volatile("" : "+v"(xf[0]), "+v"(xf[1]), "+v"(xf[2]), "+v"(xf[3]), "+v"(xf[4]));
  float sq = 0.f;
#pragma unroll
  for (int k = 0; k < 5; ++k)
#pragma unroll
    for (int e = 0; e < 4; ++e) { const float u = bflo(xf[k][e]) - mean, v = bfhi(xf[k][e]) - mean; sq = fmaf(u, u, fmaf(v, v, sq)); }
  sq = sumq(sq);
  if (lane < 16) lw4(red + 512 + wave * 64 + lane * 4, f2u(sq));
  if (wave == 0) welcome(c);
  wait_welcome();
  gather_mod(c, 0, 320, mod);
  bar();
  float var = 0.f;
#pragma unroll
  for (int k = 0; k < 4; ++k) var += u2f(lr4(red + 512 + (tt * 4 + k) * 64 + cc * 4));
  const float rstd = __builtin_amdgcn_rsqf(fmaf(var, 1.f / DM, LN_EPS));
  asm volatile("" : "+v"(xf[0]), "+v"(xf[1]), "+v"(xf[2]), "+v"(xf[3]), "+v"(xf[4]));
#pragma unroll
  for (int k = 0; k < 5; ++k) {
    const u32 col = (k0 + k) * 32 + Q * 8;
    const u32x4 sa0 = lr16(mod + col * 4), sa1 = lr16(mod + col * 4, 16);
    const u32x4 ca0 = lr16(mod + col * 4, 2560), ca1 = lr16(mod + col * 4, 2576);
    u32x4 o;
#pragma unroll
    for (int e = 0; e < 4; ++e) {
      const u32 sl = e < 2 ? sa0[2 * e] : sa1[2 * e - 4], sh = e < 2 ? sa0[2 * e + 1] : sa1[2 * e - 3];
      const u32 cl = e < 2 ? ca0[2 * e] : ca1[2 * e - 4], ch = e < 2 ? ca0[2 * e + 1] : ca1[2 * e - 3];
      o[e] = cvtpk(fmaf((bflo(xf[k][e]) - mean) * rstd, u2f(cl), u2f(sl)), fmaf((bfhi(xf[k][e]) - mean) * rstd, u2f(ch), u2f(sh)));
    }
    sink(k, o);
    pin();
  }
}

DI void ln1(KP p, Ctx& c, const u32 j) {
  const u32 lane = c.lane, wave = c.wave;
  u32x4 xf[5];
  const rsrc_t xr = mkrs(fresh(p)->x, 2048 * DM * 2);
#pragma unroll
  for (int k = 0; k < 5; ++k)
    xf[k] = ld16(xr, (16 * (wave >> 2) + (lane & 15)) * (DM * 2) + (lane >> 4) * 16, j * 32 * DM * 2 + ((wave & 3) * 5 + k) * 64);
  ln_segment(c, xf, LP_RED, LP_MOD, [&](int k, u32x4 o) { st16c(c.ws, lane * 16, W_H1 + j * 40960 + (wave * 5 + k) * 1024, o); });
  STAMPW(1, 0, 13);
  drain();
  bar();
  if (wave == 0 && lane == 0) st4c(c.ws, 0, W_CTRL + C_H1 + j * 128, 0u);
  STAMPW(1, 0, 12);
}

DI void kv_role(KP p, Ctx& c, const u32 h) {
  const u32 lane = c.lane, wave = c.wave, a = wave & 3, isv = wave >> 2;
  u32x4 w[20][2];
  auto hbase = [](u32 k) { return k == 0 ? 102400u : (k - 1) * 40960u; };
  bool first = false;
  stage_dense(c, isv ? fresh(p)->wv : fresh(p)->wk, h, w, [&] {
    first = rfl(lr4(L_MISC + (c.settled ? 8 : 4))) != 0;
    if (first) { dma4(c.ws, lane * 16, W_H1 + wave * 5120, hbase(0) + wave * 5120); dma(c.ws, lane * 16, W_H1 + wave * 5120 + 4096, hbase(0) + wave * 5120 + 4096); }
  });
  STAMPW(7, 0, 14);
  if (!first) { dma4(c.ws, lane * 16, W_H1 + wave * 5120, hbase(0) + wave * 5120); dma(c.ws, lane * 16, W_H1 + wave * 5120 + 4096, hbase(0) + wave * 5120 + 4096); }
  dma4(c.ws, lane * 16, W_H1 + 40960 + wave * 5120, hbase(1) + wave * 5120); dma(c.ws, lane * 16, W_H1 + 40960 + wave * 5120 + 4096, hbase(1) + wave * 5120 + 4096);
  asm volatile("s_waitcnt vmcnt(5)" ::: "memory");
  const u32 flag = W_CTRL + C_KV + h * 128;
  STAMPW(1, 0, 13);
  bar();
  constexpr int W = 5;
  u32x4 f[W];
  f32x4 d0[2], d1[2];
  d0[1] = zero4(); d1[1] = zero4();
  u32 cur = hbase(0) + lane * 16, k3 = 0, dstp = 0;
#pragma unroll
  for (int k = 0; k < W; ++k) f[k] = lr16(cur, k * 1024);
  auto put = [&](int t, u32 vo, u32 so) {
    st16c(c.ws, vo, so, u32x4{cvtpk(d0[t][0], d1[t][0]), cvtpk(d0[t][1], d1[t][1]), cvtpk(d0[t][2], d1[t][2]), cvtpk(d0[t][3], d1[t][3])});
  };
  for (int s = 0; s < NSEG; ++s) {
    STAMPE(1, s, 0);
    const u32 kn = k3 == 2 ? 0 : k3 + 1, kp = k3 == 0 ? 2 : k3 - 1;
    u32 hsrc = W_H1 + (u32)(s + 2 < NSEG ? s + 2 : s) * 40960 + wave * 5120;
    u32 hdst = rfl(hbase(kp) + wave * 5120);
    u32 dst = W_KV + hslot(h, s) * 16384 + isv * 8192 + a * 1024;
    u32 nxt = hbase(kn) + lane * 16;
    asm volatile("" : "+v"(cur), "+v"(nxt));
    asm volatile("" : "+s"(hsrc), "+s"(hdst), "+s"(dst));
    bool stop = false;
#pragma unroll
    for (int k = 0; k < 40; ++k) {
      const int t = k / 20, ks = k % 20;
      const f32x4 z = {0.f, 0.f, 0.f, 0.f};
      d0[t] = mfma32(w[ks][0], f[k % W], ks ? d0[t] : z); d1[t] = mfma32(w[ks][1], f[k % W], ks ? d1[t] : z);
      f[k % W] = k + W < 40 ? lr16(cur, (k + W) * 1024) : lr16(nxt, (k + W - 40) * 1024);
      if (k == 3) put(1, s > 0 ? lane * 16 : OOB, dstp);
      if (k == KV_SYNC) {
        if (s == 8 && wave == 0) wait_ge(c, lane * 128, W_CTRL + C_H1, 0);
        if (s == 1 && wave == 0) { int unused; lw4(L_MISC + 24, roles_ok(c, C_H1 + (lane & 15) * 128, unused) ? 0u : 1u); asm volatile("s_waitcnt lgkmcnt(0)" ::: "memory"); }
        if (s < KV_EAGER) drain();
        asm volatile("s_waitcnt vmcnt(1)\n\ts_barrier" ::: "memory");
        if (wave == 7) st4c(c.ws, lane == 0 ? 0 : OOB, flag, (u32)(s < KV_EAGER ? s - 1 : s - 2));
        if (s == 1) stop = rfl(lr4(L_MISC + 24)) != 0;
      }
      if (k > KV_SYNC && (k - KV_SYNC) % 4 == 1 && (k - KV_SYNC) / 4 < 5) dma(c.ws, lane * 16, hsrc + ((k - KV_SYNC) / 4) * 1024, hdst + ((k - KV_SYNC) / 4) * 1024);
      if (k == 23) put(0, lane * 16, dst);
      if (KV_PRIO && (k + 40 - KV_SYNC) % 10 == 0) {
        switch (((k + 40 - KV_SYNC) % 40) / 10) {
          case 0: __builtin_amdgcn_s_setprio(3); break;
          case 1: __builtin_amdgcn_s_setprio(2); break;
          case 2: __builtin_amdgcn_s_setprio(1); break;
          default: __builtin_amdgcn_s_setprio(0); break;
        }
      }
      pin();
    }
    if (stop) { drain(); return; }
    dstp = dst + 4096; cur = nxt; k3 = kn;
  }
  put(1, lane * 16, dstp);
  drain();
  bar();
  if (wave == 0 && lane == 0) st4c(c.ws, 0, flag, (u32)(NSEG - 1));
  finish(c);
}

constexpr u32 LQ_H1 = 0, LQ_KV = 81920, LQ_Q = 114688, LQ_KM = 131072;
DI void q_role(KP p, Ctx& c, const u32 h) {
  const u32 lane = c.lane, wave = c.wave, a = wave & 3, Q = lane >> 4, cc = lane & 15;
  if (wave < 4) {
    pace(c.t0, T_START_Q);
    u32x4 w[20][2];
    load_dense<20>(mkrs(fresh(p)->wq, DM * HD * 2), HD * 2, (h * DH + 32 * a) * 2, 0, 32, lane, w, 20);
    for (int s = 0; s < NSEG; ++s) {
      if (s == 8) wait_ge(c, lane * 128, W_CTRL + C_H1, 0);
      drain();
      bar();
      const u32 hsrc = W_H1 + (u32)(s + 1 < NSEG ? s + 1 : s) * 40960 + a * 10240, hdst = LQ_H1 + (u32)((s + 1) & 1) * 40960 + a * 10240;
      const u32 dst = LQ_Q + (u32)(s & 1) * 8192 + a * 1024 + lane * 16;
      f32x4 d0[2], d1[2];
      lds_stream<40, 6>(LQ_H1 + (u32)(s & 1) * 40960 + lane * 16, [&](int k, u32x4 f) {
        const int t = k / 20, ks = k % 20;
        const f32x4 z = {0.f, 0.f, 0.f, 0.f};
        d0[t] = mfma32(w[ks][0], f, ks ? d0[t] : z); d1[t] = mfma32(w[ks][1], f, ks ? d1[t] : z);
        if (k >= 3 && k < 33 && k % 3 == 0) dma(c.ws, lane * 16, hsrc + (k / 3 - 1) * 1024, hdst + (k / 3 - 1) * 1024);
        if (k == 23) lw16(dst, u32x4{cvtpk(d0[0][0], d1[0][0]), cvtpk(d0[0][1], d1[0][1]), cvtpk(d0[0][2], d1[0][2]), cvtpk(d0[0][3], d1[0][3])});
      });
      lw16(dst, u32x4{cvtpk(d0[1][0], d1[1][0]), cvtpk(d0[1][1], d1[1][1]), cvtpk(d0[1][2], d1[1][2]), cvtpk(d0[1][3], d1[1][3])}, 4096);
      STAMPW(1, s, 1);
    }
    drain();
    bar();
    return;
  }
  __builtin_amdgcn_s_setprio(1);
  const u32 tt = a >> 1, dh = a & 1;
  const u32 trq = cc >> 2, trp = cc & 3;
  const u32 kmb = LQ_KM + a * 3072;
  const u32 tr_lane = kmb + 256 * (trq & 1) + 128 * (trp & 1) + 8 * (4 * Q + (trq >> 1));
  const u32 vmad = kmb + 2048 + (4 * (cc >> 2) + ((cc >> 1) & 1)) * 32 + (Q & 1) * 16 + (cc & 1) * 4;
  const u32 oflag = W_CTRL + C_O + (h >> 1) * 128 + (h & 1) * 4;
  const int tok = (int)(16 * tt + cc);
  int kvr = -1;
  auto request = [&](int s) {
    const u32 mo = hslot(h, s) * 2048;
    dma2(c.ws, lane * 16, W_KM + mo, kmb); dma(c.ws, lane * 16, W_VM + mo + dh * 1024, kmb + 2048);
  };
  auto missing = [&]() {
    return __builtin_amdgcn_ballot_w64(umax4(umax4(umax4(0u, lr16(kmb + lane * 16)), lr16(kmb + lane * 16, 1024)), lr16(kmb + lane * 16, 2048)) == SENT) != 0;
  };
  wait_ge(c, (lane & 15) * 128, W_CTRL + C_H1, 0);
  dma10(c.ws, lane * 16, W_H1 + a * 10240, LQ_H1 + a * 10240);
  wait_ge<8>(c, (32 + (lane & 1) * 32 + h) * 64, W_CTRL + C_RI, 0);
  auto empty_o = [&](int s) {
    const u32x4 sent = empty4();
    st16c(c.ws, lane * 16, W_OF + hslot(h, s) * 8192 + tt * 4096 + dh * 2048, sent);
    st16c(c.ws, lane * 16 + 1024, W_OF + hslot(h, s) * 8192 + tt * 4096 + dh * 2048, sent);
  };
  empty_o(0); empty_o(1);
  drain();
  arrive(c, 4, 0, oflag, 0);
  bool behind = false;
  u32 tgot = 0;
  for (int it = 0; it <= NSEG; ++it) {
    const int s = it - 1;
    bar();
    STAMPW(5, it & 63, 0);
    if (behind) request(s);
    {
      const int kn = it < NSEG ? it : NSEG - 1;
      if (kvr < kn) kvr = (int)rfl((u32)wait_ge(c, 0, W_CTRL + C_KV + h * 128, kn));
      dma4(c.ws, lane * 16, W_KV + hslot(h, kn) * 16384 + a * 4096, LQ_KV + (u32)(it & 1) * 16384 + a * 4096);
    }
    if (s >= 0) {
      const u32 kvb = LQ_KV + (u32)(s & 1) * 16384;
      f32x4 S0 = {0.f, 0.f, 0.f, 0.f}, S1 = S0, Sm = S0;
      u32x4 qf[4];
#pragma unroll
      for (int ks = 0; ks < 4; ++ks) {
        qf[ks] = lr16(LQ_Q + (u32)(s & 1) * 8192 + tt * 4096 + lane * 16, ks * 1024);
        S0 = mfma32(lr16(kvb + lane * 16, ks * 1024), qf[ks], S0);
        S1 = mfma32(lr16(kvb + lane * 16, 4096 + ks * 1024), qf[ks], S1);
      }
      const u32 vad = kvb + 8192 + dh * 2048 + (trp * 16 + 4 * Q + trq) * 16;
      u32x4 vt[4];
#pragma unroll
      for (int n = 0; n < 4; ++n) {
        const u32x2 lo = ltr(vad, (n >> 1) * 1024 + (n & 1) * 8), hi = ltr(vad, 4096 + (n >> 1) * 1024 + (n & 1) * 8);
        vt[n] = u32x4{lo.x, lo.y, hi.x, hi.y};
      }
      const float NEG = -3.0e38f;
#pragma unroll
      for (int r = 0; r < 4; ++r) {
        S0[r] = (int)(4 * Q) + r <= tok ? S0[r] : NEG;
        S1[r] = (int)(16 + 4 * Q) + r <= tok ? S1[r] : NEG;
      }
      float m = max8(S0, S1);
      asm volatile("" : "+v"(m), "+v"(S0), "+v"(S1), "+v"(vt[0]), "+v"(vt[1]), "+v"(vt[2]), "+v"(vt[3]));
      asm volatile("s_waitcnt vmcnt(4)" ::: "memory");
      if (!behind || missing()) {
        if (s > 0) pace(tgot, T_QWAIT);
        u32 n = 0;
        for (;; ++n) {
          request(s);
          drain();
          if (!missing()) break;
          if (n >= c.lim) { timed_out(c); break; }
          __builtin_amdgcn_s_sleep(2);
        }
        behind = n == 0 && !behind;
        STAMPW(5, s, 2);
      }
      tgot = now();
      STAMPW(5, s, 3);
#pragma unroll
      for (int ks = 0; ks < 4; ++ks) {
        const u32x2 ka = ltr(tr_lane, ks * 512), kb = ltr(tr_lane, ks * 512 + 16);
        Sm = mfma32(u32x4{ka.x, ka.y, kb.x, kb.y}, qf[ks], Sm);
      }
      u32x2 vm[4];
#pragma unroll
      for (int n = 0; n < 4; ++n) vm[n] = u32x2{lr4(vmad, (n >> 1) * 512 + (n & 1) * 64), lr4(vmad, (n >> 1) * 512 + (n & 1) * 64 + 8)};
      m = maxq(mx3(mx3(m, Sm[0], Sm[1]), Sm[2], Sm[3]));
      const float mc = m * SM_C;
      float e0[4], e1[4], em[4], l = 0.f;
#pragma unroll
      for (int r = 0; r < 4; ++r) {
        e0[r] = ex2(fmaf(S0[r], SM_C, -mc));
        e1[r] = ex2(fmaf(S1[r], SM_C, -mc));
        em[r] = Q < 2 ? ex2(fmaf(Sm[r], SM_C, -mc)) : 0.f;
        l += e0[r] + e1[r] + em[r];
      }
      const u32x4 P = {cvtpk(e0[0], e0[1]), cvtpk(e0[2], e0[3]), cvtpk(e1[0], e1[1]), cvtpk(e1[2], e1[3])};
      const u32x2 Pm = {cvtpk(em[0], em[1]), cvtpk(em[2], em[3])};
      const f32x4 z = {0.f, 0.f, 0.f, 0.f};
      f32x4 o[4];
#pragma unroll
      for (int n = 0; n < 4; ++n) o[n] = mfma32(vt[n], P, z);
#pragma unroll
      for (int n = 0; n < 4; ++n) o[n] = mfma16(vm[n], Pm, o[n]);
      const float inv = __builtin_amdgcn_rcpf(sumq(l));
      drain();
#pragma unroll
      for (int n = 0; n < 2; ++n)
        st16c(c.ws, lane * 16, W_OF + hslot(h, s) * 8192 + tt * 4096 + (dh * 2 + n) * 1024,
              u32x4{cvtpk(o[2 * n][0] * inv, o[2 * n][1] * inv), cvtpk(o[2 * n][2] * inv, o[2 * n][3] * inv),
                    cvtpk(o[2 * n + 1][0] * inv, o[2 * n + 1][1] * inv), cvtpk(o[2 * n + 1][2] * inv, o[2 * n + 1][3] * inv)});
      STAMPE(5, s, 4);
      if (s + 2 < NSEG) empty_o(s + 2);
    } else drain();
  }
}

template <bool ISO> DI void gemm_role(KP p, Ctx& c, const u32 cg, const u32 tt, const u32 kh) {
  constexpr int NK = ISO ? 8 : 7;
  const u32 lane = c.lane, wave = c.wave, Q = lane >> 4, cc = lane & 15;
  const int nk = ISO || wave < 6 ? NK : NK - 1;
  const u32 rp = wave & 1;
  auto x2off = [&](int s) { return W_X2 + (((u32)s * 2 + tt) * 20 + cg * 2 + rp) * 1024; };
  if (ISO && wave < 2) {
#pragma unroll
    for (int k = 0; k < 3; ++k) st16c(c.ws, lane * 16, x2off(k) + kh * X2_HALF, empty4());
    drain();
    st4c(c.ws, lane == 0 ? 0 : OOB, W_CTRL + C_X2 + ((tt * 2 + kh) * 20 + cg * 2 + rp) * 128, 0u);
  }
  if (!ISO) {
    const u32x4 sent = empty4();
    for (u32 n = cg * 8 + wave; n < NSEG * 54; n += 80) st16c(c.ws, lane * 16, W_HID + ((n / 54) * 108 + tt * 54 + n % 54) * 1024, sent);
    drain();
    arrive(c, 8, 0, W_CTRL + C_HID + (tt * 10 + cg) * 128, 0);
  }
  u32x4 wa[NK][2], wb[NK][2];
  const rsrc_t wr = ISO ? mkrs(fresh(p)->wo, HD * DM * 2) : mkrs(fresh(p)->w2, FF * DM * 2);
  load_dense<NK>(wr, DM * 2, cg * 128, ISO ? kh * 2048 + wave * 256 : wave * 32, ISO ? 32 : 256, lane, wa, nk);
  load_dense<NK>(wr, DM * 2, cg * 128 + 64, ISO ? kh * 2048 + wave * 256 : wave * 32, ISO ? 32 : 256, lane, wb, nk);
  const u32 lsig = 14336 + rp * 4;
  bar();
  if (wave == 2 || wave == 3) lw4(lsig, SENT);
  gather_mod(c, (ISO ? 2 : 5) * DM + cg * 64, 16, LP_MOD);
  bar();
  const u32x4 g0 = lr16(LP_MOD + rp * 128 + Q * 32), g1 = lr16(LP_MOD + rp * 128 + Q * 32 + 16);
  const rsrc_t io = ISO ? mkrs(fresh(p)->x, 2048 * DM * 2) : mkrs(fresh(p)->out, 2048 * DM * 2);
  const u32 vo_row = (16 * tt + cc) * (DM * 2) + Q * 16;
  constexpr u32 LO = 16384;
  if (ISO) wait_ge<8>(c, (lane & 1) * 4, W_CTRL + C_O + (kh * 8 + wave) * 128, 0);
  else wait_ge<8>(c, (lane < 10 ? lane : 0) * 128, W_CTRL + C_HID + tt * 10 * 128, 0);
  auto canary = [&](int s) -> u32 {
    const u32 wv = (wave == 2 || wave == 3) && lane >= 32 ? wave - 2 : wave, l = lane & 31;
    if (ISO) return W_OF + hslot(kh * 16 + wv * 2 + ((l >> 1) & 1), s) * 8192 + tt * 4096 + (l & 1) * 2048 + 2044;
    return W_HID + (((u32)s * 2 + tt) * 54 + wv + 8 * ((int)l < nk ? l : 0)) * 1024 + 1020;
  };
  const u32 lbox = 8192 + wave * 256, lres = 10240 + rp * 2048, lf = 81920 + wave * 8192;
  auto request = [&](int s) {
    if (s + 1 < NSEG) peek(c, canary(s + 1), 0, lbox);
    if (ISO) {
      if (wave < 2 && kh == 0) dmaw(io, vo_row, (u32)s * 32 * DM * 2 + (cg * 2 + rp) * 64, lres);
      dma4(c.ws, lane * 16, rfl(W_OF + hslot(kh * 16 + wave * 2, s) * 8192 + tt * 4096), lf);
      dma4(c.ws, lane * 16, rfl(W_OF + hslot(kh * 16 + wave * 2 + 1, s) * 8192 + tt * 4096), lf + 4096);
    } else {
      u32 so = rfl(W_HID + (((u32)s * 2 + tt) * 54 + wave) * 1024), xo = rfl(x2off(s));
      asm volatile("" : "+s"(so), "+s"(xo));
      if (wave < 2) { dma(c.ws, lane * 16, xo, lres); dma(c.ws, lane * 16, xo + X2_HALF, lres + 1024); }
#pragma unroll
      for (int k = 0; k < 7; ++k) dma(c.ws, lane * 16, k < nk ? so + k * 8192 : so, lf + k * 1024);
    }
  };
  if (ISO && wave < 2 && kh) lw16(lres + lane * 16, u32x4{});
  bool pre = false;
  for (int s = 0; s < NSEG; ++s) {
    if (!pre) {
      if (wave < 2) {
        while ((int)rfl(__hip_atomic_load((LAS u32*)lp(lsig, 0), __ATOMIC_RELAXED, __HIP_MEMORY_SCOPE_WORKGROUP)) < s) __builtin_amdgcn_s_sleep(1);
      } else wait_set<G_SLP>(c, canary(s));
    }
    if (wave == 2 || wave == 3) __hip_atomic_store((LAS u32*)lp(lsig, 0), (u32)s, __ATOMIC_RELAXED, __HIP_MEMORY_SCOPE_WORKGROUP);
    request(s);
    STAMPE(0, s, 0);
    f32x4 d[4];
    for (u32 tries = 0;; ++tries) {
      u32x4 f[NK];
      auto landed = [&](auto n) { asm volatile("s_waitcnt vmcnt(%0)" :: "i"(n.value) : "memory"); };
      landed(ic<NK - 1>{});
      f[0] = lr16(lf + lane * 16);
      unroll<NK>([&](auto kc) {
        constexpr int k = kc.value;
        if constexpr (k + 1 < NK) { landed(ic<NK - 2 - k>{}); f[k + 1] = lr16(lf + lane * 16, (k + 1) * 1024); }
        pin();
        const f32x4 z = {0.f, 0.f, 0.f, 0.f};
        d[0] = mfma32(wa[k][0], f[k], k ? d[0] : z); d[1] = mfma32(wa[k][1], f[k], k ? d[1] : z);
        d[2] = mfma32(wb[k][0], f[k], k ? d[2] : z); d[3] = mfma32(wb[k][1], f[k], k ? d[3] : z);
        pin();
      });
      const float d00 = d[0][0];
      if (__builtin_expect(__builtin_amdgcn_ballot_w64(d00 != d00) == 0, 1)) break;
      u32 m = 0;
#pragma unroll
      for (int k = 0; k < NK; ++k) m = umax4(m, lr16(lf + lane * 16, k * 1024));
      if (__builtin_amdgcn_ballot_w64(m == SENT) == 0) break;
      if (tries >= c.lim) { timed_out(c); break; }
      request(s);
    }
    pre = s + 1 < NSEG && __builtin_amdgcn_ballot_w64(lr4(lbox + lane * 4) == SENT) == 0;
    const u32 lb = LO + (u32)(s & 1) * 32768;
#pragma unroll
    for (int n = 0; n < 4; ++n) lw16(lb + wave * 4096 + lane * 16, __builtin_bit_cast(u32x4, d[n]), n * 1024);
    bar();
    if (wave < 2) {
      const u32x4 ra = lr16(lres + lane * 16), rb = lr16(lres + lane * 16, 1024);
      f32x4 t0 = {0.f, 0.f, 0.f, 0.f}, t1 = t0;
#pragma unroll
      for (int k = 0; k < 8; ++k) {
        t0 += __builtin_bit_cast(f32x4, lr16(lb + rp * 2048 + lane * 16, k * 4096));
        t1 += __builtin_bit_cast(f32x4, lr16(lb + rp * 2048 + lane * 16, k * 4096 + 1024));
      }
      u32x4 o;
#pragma unroll
      for (int r = 0; r < 4; ++r) {
        const u32 gl = r < 2 ? g0[2 * r] : g1[2 * r - 4], gh = r < 2 ? g0[2 * r + 1] : g1[2 * r - 3];
        if (ISO) o[r] = guard(cvtpk(fmaf(u2f(gl), t0[r], bflo(ra[r])), fmaf(u2f(gh), t1[r], bfhi(ra[r]))));
        else o[r] = cvtpk(fmaf(u2f(gl), t0[r], bflo(ra[r]) + bflo(rb[r])), fmaf(u2f(gh), t1[r], bfhi(ra[r]) + bfhi(rb[r])));
      }
      if (ISO) {
        st16c(c.ws, lane * 16, x2off(s) + kh * X2_HALF, o);
        st16c(c.ws, s + 3 < NSEG ? lane * 16 : OOB, x2off(s + 3) + kh * X2_HALF, empty4());
      } else __builtin_amdgcn_raw_buffer_store_b128(o, io, vo_row, (u32)s * 32 * DM * 2 + (cg * 2 + rp) * 64, 0);
      STAMPW(0, s, 1);
    }
  }
}

DI void f1_role(KP p, Ctx& c, const u32 cg, const u32 tt, const u32 par) {
  const u32 lane = c.lane, wave = c.wave, Q = lane >> 4, cc = lane & 15;
  const u32 pi = cg * 8 + wave;
  auto hoff = [&](int s) { return W_HID + (((u32)s * 2 + tt) * 54 + pi) * 1024; };
  const u32 vo_h = pi < 54 ? lane * 16 : OOB;
  u32x4 w[20][2];
  load_dense<20>(mkrs(fresh(p)->w1, DM * FF * 2), FF * 2, pi * 64, 0, 32, lane, w, pi < 54 ? 20 : 0);
  bar();
  gather_mod(c, 3 * DM, 320, LP_MOD);
  constexpr u32 LH = 16384;
  wait_ge<8>(c, lane < 40 ? C_X2 + (tt * 40 + lane) * 128 : C_HID + (tt * 10 + (lane < 50 ? lane - 40 : 0)) * 128, W_CTRL, 0);
  u32 tseen = now();
  for (int s = (int)par; s < NSEG; s += 2) {
    const u32 src = W_X2 + ((u32)s * 2 + tt) * 20480;
    if (wave == 1) {
      pace(tseen, F1_WAIT);
      const u32 n = wait_set<F1_SLP>(c, src + (lane & 1) * X2_HALF + 1020);
      tseen = now();
      if (n > 0 && s < NSEG - 2) pace(tseen, F1_DELAY);
    }
    bar();
    STAMPE(0, s, 0);
    f32x2 x[12];
    for (u32 tries = 0;; ++tries) {
      u32x4 xa[3], xb[3];
      asm volatile("" ::: "memory");
#pragma unroll
      for (int k = 0; k < 3; ++k) {
        const u32 vo = (k < 2 || wave < 4) ? lane * 16 : OOB;
        xa[k] = ld16c(c.ws, vo, src + wave * 1024 + k * 8192);
        xb[k] = ld16c(c.ws, vo, src + wave * 1024 + k * 8192 + X2_HALF);
      }
      u32 m = 0;
#pragma unroll
      for (int k = 0; k < 3; ++k) {
        m = umax4(umax4(m, xa[k]), xb[k]);
#pragma unroll
        for (int e = 0; e < 4; ++e) x[4 * k + e] = f32x2{bflo(xa[k][e]), bfhi(xa[k][e])} + f32x2{bflo(xb[k][e]), bfhi(xb[k][e])};
      }
      if (__builtin_expect(__builtin_amdgcn_ballot_w64(m == SENT) == 0, 1)) break;
      if (tries >= c.lim) { timed_out(c); break; }
    }
    f32x2 s2 = ((x[0] + x[1]) + (x[2] + x[3])) + ((x[4] + x[5]) + (x[6] + x[7])) + ((x[8] + x[9]) + (x[10] + x[11]));
    const float lm = sumq(s2.x + s2.y) * (wave < 4 ? 1.f / 96.f : 1.f / 64.f);
    f32x2 qa = {0.f, 0.f}, qb = qa;
#pragma unroll
    for (int e = 0; e < 8; ++e) { const f32x2 u = x[e] - lm; qa = u * u + qa; }
#pragma unroll
    for (int e = 8; e < 12; ++e) { const f32x2 u = x[e] - lm; qb = u * u + qb; }
    const float sq = sumq(qa.x + qa.y + (wave < 4 ? qb.x + qb.y : 0.f));
    if (lane < 16) lw8(LP_RED + wave * 128 + lane * 8, u32x2{f2u(lm), f2u(sq)});
    bar();
    float mean, m2;
    {
      const u32x2 va = lr8(LP_RED + Q * 128 + cc * 8), vb = lr8(LP_RED + Q * 128 + cc * 8, 512);
      mean = sumq(fmaf(u2f(va.x), 96.f / DM, u2f(vb.x) * (64.f / DM)));
      const float da = u2f(va.x) - mean, db = u2f(vb.x) - mean;
      m2 = sumq(fmaf(da * da, 96.f, fmaf(db * db, 64.f, u2f(va.y) + u2f(vb.y))));
    }
    const float rstd = __builtin_amdgcn_rsqf(fmaf(m2, 1.f / DM, LN_EPS)), nm = -mean * rstd;
#pragma unroll
    for (int k = 0; k < 3; ++k) {
      if (k < 2 || wave < 4) {
        const u32 ma = LP_MOD + (wave * 32 + Q * 8) * 4;
        const u32x4 sa0 = lr16(ma, k * 1024), sa1 = lr16(ma, k * 1024 + 16), ca0 = lr16(ma, k * 1024 + 2560), ca1 = lr16(ma, k * 1024 + 2576);
        u32x4 o;
#pragma unroll
        for (int e = 0; e < 4; ++e) {
          const f32x2 sh = e < 2 ? f32x2{u2f(sa0[2 * e]), u2f(sa0[2 * e + 1])} : f32x2{u2f(sa1[2 * e - 4]), u2f(sa1[2 * e - 3])};
          const f32x2 sc = e < 2 ? f32x2{u2f(ca0[2 * e]), u2f(ca0[2 * e + 1])} : f32x2{u2f(ca1[2 * e - 4]), u2f(ca1[2 * e - 3])};
          const f32x2 y = (x[4 * k + e] * rstd + nm) * sc + sh;
          o[e] = cvtpk(y.x, y.y);
        }
        lw16(LH + wave * 1024 + lane * 16, o, k * 8192);
      }
    }
    bar();
    f32x4 d0, d1;
    lds_stream<20, 6>(LH + lane * 16, [&](int k, u32x4 f) {
      const f32x4 z = {0.f, 0.f, 0.f, 0.f};
      d0 = mfma32(w[k][0], f, k ? d0 : z); d1 = mfma32(w[k][1], f, k ? d1 : z);
    });
    u32x4 o;
#pragma unroll
    for (int r = 0; r < 4; ++r) {
      const float u = d0[r], v = d1[r];
      o[r] = cvtpk(u * __builtin_amdgcn_rcpf(1.f + __expf(-u)), v * __builtin_amdgcn_rcpf(1.f + __expf(-v)));
    }
    st16c(c.ws, vo_h, hoff(s), o);
    STAMPW(0, s, 1);
  }
}

DI void probe(Ctx& c, u32 k, u32 half, u32 (&best)[2]) {
#pragma unroll 1
  for (int it = 0; it < 8; ++it) {
    const u32 t0 = now();
    st4c(c.ws, c.lane == 0 ? 0 : OOB, ((it & 1) ? W_ZM : W_PART) + W_PROBE + half * 8192 + (k & 1) * 4096 + k * 256, 0u);
    drain();
    const u32 d = now() - t0;
    if (it & 1) best[1] = min(best[1], d); else best[0] = min(best[0], d);
  }
}

DI bool attempt(KP p, Ctx& c, const u32 x, const bool greeted) {
  const u32 id = blockIdx.x >> 3, y = x & 3, hd = y * 8 + (id & 7);
  if (x < 4 && id >= 24) {
    if (c.wave == 0) {
      drain();
      if (id < 28) {
        u32 best[2] = {0x7FFFFFFFu, 0x7FFFFFFFu};
        probe(c, y * 2 + ((id >> 1) & 1), (id ^ (c.xcc >> 2)) & 1, best);
        lw8(LP_RED, u32x2{best[0], best[1]});
      }
      if (!greeted) welcome(c);
    }
    bar();
  } else {
    if (x < 4 || id < 8) {
      if (x >= 4) { c.lim = SPIN_SLOW; kv_role(p, c, hd); }
      else if (pace(c.t0, T_START_R), id < 8) rec_role<R_X>(p, c, hd);
      else if (id < 16) rec_role<R_P>(p, c, hd);
      else rec_role<R_V>(p, c, hd);
      return false;
    }
    if (id < 16) {
      const u32 q = (id - 8) * 4 + y;
      mod_slab(p, c, q, true);
      if (q < 28) mod_slab(p, c, 32 + q, false);
    } else ln1(p, c, (id - 16) * 4 + y);
  }
  int unused;
  return roles_ok(c, OOB, unused);
}

DI void rebuild(KP kp, Ctx& c) {
  c.wsp = fresh(kp)->ws;
  c.ws = mkrs(c.wsp, W_END);
  asm volatile("v_mbcnt_lo_u32_b32 %0, -1, 0\n\tv_mbcnt_hi_u32_b32 %0, -1, %0" : "=v"(c.lane));
  asm volatile("" : "+s"(c.wave));
}

__global__ void __launch_bounds__(512, 2) block_kernel(Params p) {
  extern __shared__ __attribute__((aligned(16))) char lds_raw[];
  const KP kp = (KP)__builtin_amdgcn_kernarg_segment_ptr();
  Ctx c;
  c.wsp = p.ws;
  c.ws = mkrs(p.ws, W_END);
  c.lane = threadIdx.x & 63;
  c.wave = rfl(threadIdx.x >> 6);
  c.left = 1;
  c.t0 = now();
  c.xcc = (u32)__builtin_amdgcn_s_getreg(20 | (3 << 11)) & 7u;
  c.nonce = 0;
  c.settled = false;
  c.lim = SPIN_FAST;
  if (TOUCH && c.wave >= 6) {
    u64 pc; u32 bytes;
    asm volatile("s_getpc_b64 %0\n.Lblock_pc:\n\ts_mov_b32 %1, .Lafter_pc-.Lblock_pc" : "=s"(pc), "=s"(bytes));
    if (bytes < MB) {
      const u32 vo = (((c.wave - 6) * 64 + c.lane) * 32 + (blockIdx.x >> 3)) * 64;
      Ctx t; t.ws = mkrs((const void*)pc, bytes);
      peek(t, vo + 4 <= bytes ? vo : 0u, 0, L_MISC + 768);
    }
  }
  if (threadIdx.x < 8) *(LAS u32*)(lds_raw + L_MISC + threadIdx.x * 4) = 0;
  bar();
  if (c.wave == 0) hello(c);
  bool greeted = false;
  if (blockIdx.x == 255) {
    const u32x4 sent = {SENT, SENT, SENT, SENT};
#pragma unroll
    for (int k = 0; k < 8; ++k) st16c(c.ws, threadIdx.x * 16, W_CTRL + k * 8192, sent);
    drain();
    bar();
    if (c.wave == 0) welcome(c);
    if (c.wave >= 4) {
      const u32 j = threadIdx.x - 256;
      u32 last = 0;
      for (u32 k = 0; k < c.lim; ++k) {
        u32 ch; int t;
        asm volatile("s_nop 4\n\tbuffer_load_dword %0, %2, %4, %5 offen sc1\n\tbuffer_load_dword %1, %3, %4, %6 offen sc1\n\ts_waitcnt vmcnt(0)"
                     : "=&v"(ch), "=&v"(t) : "v"(j * 256), "v"((j >> 3) * 128 + (j & 7) * 4), "s"(c.ws), "s"(W_HS), "s"(W_CTRL + C_T) : "memory");
        st4c(c.ws, (k == 0 || ch != last) ? j * 256 + 128 : OOB, W_HS, ch);
        last = ch;
        if (__builtin_amdgcn_ballot_w64(t < 0) == 0) break;
      }
    }
    bar();
    greeted = true;
  }
  u32 x = c.xcc;
  if (!attempt(kp, c, x, greeted)) {
    bar();
    rebuild(kp, c);
    if (c.wave == 0 && c.lane < 5) lw4(L_MISC + 12 + c.lane * 4, 0u);
    bar();
    x = blockIdx.x & 7;
    c.left = 1; c.xcc = x; c.settled = true; c.lim = SPIN_FAST;
    attempt(kp, c, x, true);
  }
  rebuild(kp, c);
  x = rfl(x);
  asm volatile("" : "+s"(x));
  c.lim = SPIN_SLOW;
  const u32 id = blockIdx.x >> 3, y = x & 3;
  if (x < 4) {
    if (c.wave == 0 && id < 28)
      st4c(c.ws, c.lane < 2 ? c.lane * 64 : OOB, W_CTRL + C_PAR + ((id & 1) * 8 + y * 2 + ((id >> 1) & 1)) * 4, lr4(LP_RED + (c.lane & 1) * 4));
    pace(c.t0, T_START_POST);
    if (NO_WORK == 0 && id < 31) f1_role(kp, c, id - 24, y >> 1, y & 1);
  } else if (id < 16) {
    if (NO_WORK != 1) q_role(kp, c, y * 8 + (id & 7));
  } else {
    pace(c.t0, id < 26 ? T_START_O : T_START_POST);
    if (NO_WORK == 1 || NO_WORK == 2 || (NO_WORK == 3 && id >= 26)) {}
    else if (id < 26) gemm_role<true>(kp, c, id - 16, y >> 1, y & 1);
    else if (id < 31) gemm_role<false>(kp, c, (y & 1) * 5 + (id - 26), y >> 1, 0);
  }
  finish(c);
}

__global__ void text_end_kernel() { asm volatile(".Lafter_pc:"); }

}

extern "C" void kernel_launch(void* const* d_in, const int* in_sizes, int n_in, void* d_out, int out_size, void* d_ws,
                              size_t ws_size, hipStream_t stream) {
  (void)in_sizes; (void)n_in; (void)out_size; (void)ws_size;
  char* base = (char*)(((uintptr_t)d_ws + 2 * MB - 1) & ~(uintptr_t)(2 * MB - 1));
  Params p;
  p.x = d_in[0]; p.mem0 = d_in[1]; p.wq = d_in[2]; p.wk = d_in[3]; p.wv = d_in[4]; p.wo = d_in[5];
  p.w1 = d_in[8]; p.w2 = d_in[9]; p.c = d_in[10]; p.wmod = d_in[11];
  p.out = d_out; p.ws = base;
  (void)hipFuncSetAttribute((const void*)block_kernel, hipFuncAttributeMaxDynamicSharedMemorySize, LDS_BYTES);
  block_kernel<<<256, 512, LDS_BYTES, stream>>>(p);
  if (d_in == nullptr) text_end_kernel<<<1, 64, 0, stream>>>();
}
```
